```python
import torch

def _softmax(x, axis=-1):
    return torch.softmax(x, dim=axis)
def _gelu(x):
    return torch.nn.functional.gelu(x, approximate="tanh")
def _silu(x):
    return torch.nn.functional.silu(x)
def _sigmoid(x):
    return torch.sigmoid(x)
def _layernorm(x, axis=-1, eps=1e-5):
    m = x.mean(dim=axis, keepdim=True)
    v = x.var(dim=axis, keepdim=True, unbiased=False)
    return (x - m) / torch.sqrt(v + eps)
def _scatter_add(tgt, idx, src, axis=0):
    return tgt.clone().index_add_(axis, idx, src)
def _segment_sum(src, seg, n):
    out = torch.zeros((n,) + src.shape[1:], dtype=src.dtype)
    return out.index_add_(0, seg, src)
def _pad(x, pw):
    flat = tuple(v for pair in reversed(pw) for v in pair)
    return torch.nn.functional.pad(x, flat)

kernel_name = 'gated_mix__spectral_fnet__softmax_full_mha__prompt'

def setup_inputs():

    _w = (lambda a: (lambda t: t.to(torch.bfloat16) if t.is_floating_point() else t)(torch.as_tensor(a)))
    def _A_setup():
        B, S, D = 1, 2048, 640
        ns = torch.arange(S)[:, None] * torch.arange(S)[None, :]
        nd = torch.arange(D)[:, None] * torch.arange(D)[None, :]
        Ws_re = torch.cos(-2.0 * torch.pi * ns / S).to(float)
        Ws_im = torch.sin(-2.0 * torch.pi * ns / S).to(float)
        Wd_re = torch.cos(-2.0 * torch.pi * nd / D).to(float)
        Wd_im = torch.sin(-2.0 * torch.pi * nd / D).to(float)
        return {
            "x": _w((lambda *s: torch.randn(*s, dtype=torch.bfloat16) * 0.05)(B, S, D)),
            "dft_seq_re": _w(Ws_re),
            "dft_seq_im": _w(Ws_im),
            "dft_hid_re": _w(Wd_re),
            "dft_hid_im": _w(Wd_im),
        }
    _A_in = _A_setup()

    def _B_setup():
        B, S, D = 1, 2048, 640
        return {
            "x": _w((lambda *s: torch.randn(*s, dtype=torch.bfloat16) * 0.05)(B, S, D)),
            "wqkv": _w((lambda *s: torch.randn(*s, dtype=torch.bfloat16) * 1.6)(D, 3 * D)),
            "wo": _w((lambda *s: torch.randn(*s, dtype=torch.bfloat16) * 1.6)(D, D)),
        }
    _B_in = _B_setup()

    return {
        "A_x": _A_in["x"],
        "A_dft_seq_re": _A_in["dft_seq_re"],
        "A_dft_seq_im": _A_in["dft_seq_im"],
        "A_dft_hid_re": _A_in["dft_hid_re"],
        "A_dft_hid_im": _A_in["dft_hid_im"],
        "B_x": _B_in["x"],
        "B_wqkv": _B_in["wqkv"],
        "B_wo": _B_in["wo"],
        "gate_w": _w((lambda *s: torch.randn(*s, dtype=torch.bfloat16) * 0.05)(640) * 16.0),
    }

def reference(A_x, A_dft_seq_re, A_dft_seq_im, A_dft_hid_re, A_dft_hid_im, B_x, B_wqkv, B_wo, gate_w):
    def _A_ref(x, dft_seq_re, dft_seq_im, dft_hid_re, dft_hid_im):
        a = torch.einsum("bsd,ed->bse", x, dft_hid_re)
        b = torch.einsum("bsd,ed->bse", x, dft_hid_im)
        y = torch.einsum("ts,bse->bte", dft_seq_re, a) - torch.einsum("ts,bse->bte", dft_seq_im, b)
        return y

    def _B_ref(x, wqkv, wo):
        B, S, D = x.shape
        H = 32; Dh = D // H
        qkv = (lambda x, *d: x.permute(*d))((x @ wqkv).reshape(B, S, 3, H, Dh), 2, 0, 3, 1, 4)
        q, k, v = qkv[0], qkv[1], qkv[2]
        scale = Dh ** -0.5
        scores = torch.einsum("bhqd,bhkd->bhqk", q, k) * scale
        p = _softmax(scores, axis=-1)
        o = torch.einsum("bhqk,bhkd->bhqd", p, v)
        return (lambda x, *d: x.permute(*d))(o, 0, 2, 1, 3).reshape(B, S, D) @ wo

    x = A_x
    g = _sigmoid((x * gate_w).sum(-1, keepdims=True))
    a = _A_ref(x, A_dft_seq_re, A_dft_seq_im, A_dft_hid_re, A_dft_hid_im)
    b = _B_ref(x, B_wqkv, B_wo)
    return g * a + (1 - g) * b
```

```cpp
#include <hip/hip_runtime.h>
#include <hip/hip_bf16.h>
#include <stdint.h>

typedef __attribute__((ext_vector_type(8))) __bf16 bf16x8;
typedef __attribute__((ext_vector_type(2))) __bf16 bf16x2;
typedef __attribute__((ext_vector_type(4))) short s16x4;
typedef __attribute__((ext_vector_type(16))) float f32x16;
typedef __attribute__((ext_vector_type(4))) float f32x4;
typedef __attribute__((ext_vector_type(2))) float f32x2;
typedef __attribute__((ext_vector_type(4))) unsigned u32x4;
typedef __attribute__((ext_vector_type(2))) unsigned u32x2;
#define LAS __attribute__((address_space(3)))
#define DI __device__ __forceinline__
#define SBAR() __builtin_amdgcn_sched_barrier(0)
#define PIN(x) asm volatile("" : "+v"(x))

constexpr unsigned REC = 3072, KV_HS = 65 * REC;
constexpr unsigned WS_KV = 0;
constexpr unsigned WS_Q = WS_KV + 32 * KV_HS;
constexpr unsigned WS_AB = WS_Q + 32 * 64 * 1280;
constexpr unsigned WS_P = WS_AB + 2 * 4 * 128 * 5 * 1024;
constexpr unsigned WS_O1 = WS_P + 4 * 64 * 80 * 512;
constexpr unsigned WS_O2 = WS_O1 + 64 * 32 * 1024;
constexpr unsigned WS_WT = WS_O2 + 64 * 32 * 256;
constexpr unsigned WS_G = WS_WT + 800 * 1024;
constexpr unsigned WS_END = WS_G + 8192;
constexpr float QSCALE = 0.22360679774997896f * 1.4426950408889634f;

DI unsigned swap23(unsigned r) { return (r & ~12u) | ((r & 4u) << 1) | ((r & 8u) >> 1); }
DI unsigned pk2(float a, float b) { f32x2 v = {a, b}; return __builtin_bit_cast(unsigned, __builtin_convertvector(v, bf16x2)); }
DI float bflo(unsigned u) { return __uint_as_float(u << 16); }
DI float bfhi(unsigned u) { return __uint_as_float(u & 0xffff0000u); }
DI f32x16 mma32(u32x4 a, u32x4 b, f32x16 c) { return __builtin_amdgcn_mfma_f32_32x32x16_bf16(__builtin_bit_cast(bf16x8, a), __builtin_bit_cast(bf16x8, b), c, 0, 0, 0); }
DI f32x16 mma32k8(u32x2 a, u32x2 b, f32x16 c) { return __builtin_amdgcn_mfma_f32_32x32x8bf16_1k(__builtin_bit_cast(s16x4, a), __builtin_bit_cast(s16x4, b), c, 0, 0, 0); }
DI f32x4 mma16(u32x4 a, u32x4 b, f32x4 c) { return __builtin_amdgcn_mfma_f32_16x16x32_bf16(__builtin_bit_cast(bf16x8, a), __builtin_bit_cast(bf16x8, b), c, 0, 0, 0); }
DI u32x4 lds128(unsigned a) { return *(const LAS u32x4*)(uintptr_t)a; }
DI u32x2 lds64(unsigned a) { return *(const LAS u32x2*)(uintptr_t)a; }
DI u32x2 ldstr(unsigned a) { return __builtin_bit_cast(u32x2, __builtin_amdgcn_ds_read_tr16_b64_v4i16((LAS s16x4*)(uintptr_t)a)); }
DI void dma16(unsigned voff, const char* base, unsigned lds) {
    asm volatile("s_mov_b32 m0, %2\n\ts_nop 0\n\tglobal_load_lds_dwordx4 %0, %1" ::"v"(voff), "s"(base), "s"(lds) : "memory");
}
DI void dma16x3(unsigned voff, const char* base, unsigned lds) {
    asm volatile("s_mov_b32 m0, %2\n\ts_nop 0\n\tglobal_load_lds_dwordx4 %0, %1\n\tglobal_load_lds_dwordx4 %0, %1 offset:1024\n\tglobal_load_lds_dwordx4 %0, %1 offset:2048" ::"v"(voff), "s"(base), "s"(lds) : "memory");
}
#define WAITV_BAR(N) asm volatile("s_waitcnt vmcnt(" #N ")\n\ts_barrier" ::: "memory")
#define WAITL_BAR() asm volatile("s_waitcnt lgkmcnt(0)\n\ts_barrier" ::: "memory")
#define WAITVL_BAR() asm volatile("s_waitcnt vmcnt(0) lgkmcnt(0)\n\ts_barrier" ::: "memory")

constexpr unsigned K1_STAGE = 16384 + 28672, K1_LDS = 3 * K1_STAGE;

template <int T>
DI void k1_compute(char* ws, unsigned lds0, int w, int lane, int rb, int sec, int grp, int col0) {
    const unsigned r = lane & 31, hh = lane >> 5, pr = swap23(r);
    const unsigned rel = pr * 128 + ((hh ^ ((pr >> 1) & 7)) << 4);
    const unsigned xrel = lds0 + w * 4096 + rel;
    const unsigned wrel = T == 2 ? lds0 + 16384 + rel : lds0 + 16384 + (8 * hh + ((lane & 15) >> 2)) * 448 + ((lane >> 4) & 1) * 32 + (lane & 3) * 8;
    f32x16 acc[7];
#pragma unroll
    for (int j = 0; j < 7; ++j) acc[j] = f32x16{};
    u32x4 xf[2], wf[2][7];
    auto rd_x = [&](int set, unsigned sb, int ks) { xf[set] = lds128((xrel + sb) ^ (ks << 5)); };
    auto rd_w = [&](int set, unsigned sb, int ks, int j) {
        if (T == 2) wf[set][j] = lds128(((wrel + sb) ^ (ks << 5)) + j * 4096);
        else { const u32x2 lo = ldstr(wrel + sb + ks * 7168 + j * 64), hi = ldstr(wrel + sb + ks * 7168 + 1792 + j * 64); wf[set][j] = (u32x4){lo[0], lo[1], hi[0], hi[1]}; }
    };
    WAITL_BAR();
    rd_x(0, 0, 0);
#pragma unroll
    for (int j = 0; j < 7; ++j) rd_w(0, 0, 0, j);
    unsigned sb = 0;
    for (int s = 0; s < 10; ++s) {
#pragma unroll
        for (int ks = 0; ks < 4; ++ks) {
            const int c = ks & 1, n = c ^ 1, nks = (ks + 1) & 3;
            if (ks == 3) { WAITL_BAR(); sb = sb == 2 * K1_STAGE ? 0 : sb + K1_STAGE; }
            SBAR();
#pragma unroll
            for (int j = 0; j < 7; ++j) {
                acc[j] = T == 0 ? mma32(wf[c][j], xf[c], acc[j]) : mma32(xf[c], wf[c][j], acc[j]);
                if (j == 0) { rd_x(n, sb, nks); rd_w(n, sb, nks, 0); }
                if (T == 2) { if (j >= 1 && j <= 3) { rd_w(n, sb, nks, 2 * j - 1); rd_w(n, sb, nks, 2 * j); } }
                else { if (j == 1 || j == 3) { rd_w(n, sb, nks, j == 1 ? 1 : 4); rd_w(n, sb, nks, j == 1 ? 2 : 5); } if (j == 2 || j == 4) rd_w(n, sb, nks, j == 2 ? 3 : 6); }
                SBAR();
            }
        }
    }
    const int kt = rb * 4 + w, j0 = grp == 2 ? 1 : 0;
    if (T == 0) {
        const bool isk = sec == 1;
        const unsigned hs = isk ? KV_HS : 64 * 1280, slot = isk ? r : pr;
        const float sc = isk ? 1.f : QSCALE;
        char* base = ws + (isk ? WS_KV + kt * REC : WS_Q + kt * 1280);
        unsigned off[5];
#pragma unroll
        for (int n = 0; n < 5; ++n) {
            const unsigned qd = col0 / 4 + 2 * n + hh, head = qd / 5, dq = qd - head * 5;
            off[n] = head * hs + (dq < 4 ? (dq >> 1) * 512 + slot * 16 + (dq & 1) * 8 : 1024 + slot * 8);
        }
#pragma unroll
        for (int j = 0; j < 7; ++j) {
            if (j < j0) continue;
#pragma unroll
            for (int g = 0; g < 4; ++g) {
                const int n = 4 * j + g;
                const u32x2 v = {pk2(acc[j][4 * g] * sc, acc[j][4 * g + 1] * sc), pk2(acc[j][4 * g + 2] * sc, acc[j][4 * g + 3] * sc)};
                *(u32x2*)(base + (size_t)((n / 5) * 2 * hs) + off[n % 5]) = v;
            }
        }
    } else {
        const float sc = sec == 4 ? -1.f : 1.f;
#pragma unroll
        for (int j = 0; j < 7; ++j) {
            if (j < j0) continue;
            char* p; unsigned sstep;
            if (T == 1) {
                const unsigned c = col0 + 32 * j + r, head = c / 20, d = c - head * 20;
                p = ws + WS_KV + (kt + 1) * REC + 1536 + (size_t)(head * KV_HS + hh * 384 + (d < 16 ? swap23(d) : d) * 16); sstep = 768;
            } else {
                const int ct = col0 / 32 + j, nt = ct / 5, n5 = ct - nt * 5;
                p = ws + WS_AB + (size_t)(((((sec - 3) * 4 + nt) * 128 + 2 * kt) * 5 + n5) * 1024) + lane * 16; sstep = 5120;
            }
#pragma unroll
            for (int s = 0; s < 2; ++s) {
                const f32x16& a = acc[j];
                u32x4 v;
                if (T == 1) v = (u32x4){pk2(a[8 * s], a[8 * s + 1]), pk2(a[8 * s + 2], a[8 * s + 3]), pk2(a[8 * s + 4], a[8 * s + 5]), pk2(a[8 * s + 6], a[8 * s + 7])};
                else v = (u32x4){pk2(a[8 * s] * sc, a[8 * s + 1] * sc), pk2(a[8 * s + 2] * sc, a[8 * s + 3] * sc), pk2(a[8 * s + 4] * sc, a[8 * s + 5] * sc), pk2(a[8 * s + 6] * sc, a[8 * s + 7] * sc)};
                *(u32x4*)(p + s * sstep) = v;
            }
        }
    }
}

__global__ __launch_bounds__(512, 2) void k1_proj(const char* x, const char* wr, const char* wi, const char* wqkv, const char* gw, char* ws) {
    extern __shared__ __attribute__((aligned(16))) char smem[];
    const unsigned lds0 = (unsigned)(uintptr_t)smem;
    const int lane = threadIdx.x & 63, w = __builtin_amdgcn_readfirstlane(threadIdx.x >> 6);
    const int b = blockIdx.x, xcd = b & 7, idx = b >> 3;
    const int rb = (xcd & 1) * 8 + (idx & 7), cg = (xcd >> 1) * 4 + (idx >> 3);
    if (cg == 15) {
        const int row = rb * 128 + (threadIdx.x >> 2), sub = threadIdx.x & 3;
        float s = 0.f;
#pragma unroll 5
        for (int i = 0; i < 20; ++i) {
            const u32x4 a = *(const u32x4*)(x + row * 1280 + (4 * i + sub) * 16), g = *(const u32x4*)(gw + (4 * i + sub) * 16);
#pragma unroll
            for (int e = 0; e < 4; ++e) s += bflo(a[e]) * bflo(g[e]) + bfhi(a[e]) * bfhi(g[e]);
        }
        s += __shfl_xor(s, 1); s += __shfl_xor(s, 2);
        if (sub == 0) *(float*)(ws + WS_G + row * 4) = 1.f / (1.f + __expf(-s));
#pragma unroll
        for (int i = 0; i < 4; ++i) {
            const int it = threadIdx.x + 512 * i, rec = it >> 4, head = rec >> 2, kt = rb * 4 + (rec & 3);
            *(u32x4*)(ws + WS_KV + head * KV_HS + kt * REC + 1280 + (it & 15) * 16) = (u32x4){0x00003f80u, 0u, 0x00003f80u, 0u};
        }
        { const int rec = threadIdx.x >> 2, head = rec >> 2, kt = rb * 4 + (rec & 3);
          *(u32x4*)(ws + WS_KV + head * KV_HS + (kt + 1) * REC + 1536 + sub * 384 + 320) = (u32x4){0x3f803f80u, 0x3f803f80u, 0x3f803f80u, 0x3f803f80u}; }
        return;
    }
    const int sec = cg / 3, grp = cg - sec * 3, col0 = grp == 2 ? 416 : grp * 224;
    if (w >= 4) {
        const int li = w - 4;
        unsigned xo[4], wo_[7];
#pragma unroll
        for (int i = 0; i < 4; ++i) { const unsigned row = 8 * i + (lane >> 3); xo[i] = row * 1280 + (((lane & 7) ^ ((row >> 1) & 7)) << 4); }
#pragma unroll
        for (int i = 0; i < 7; ++i) {
            if (sec >= 3) { const unsigned row = 56 * li + 8 * i + (lane >> 3); wo_[i] = row * 1280 + (((lane & 7) ^ ((row >> 1) & 7)) << 4); }
            else { const unsigned o = 64 * i + lane, kr = o / 28; wo_[i] = (16 * li + kr) * 3840 + (o - kr * 28) * 16; }
        }
        const char* xb = x + (rb * 128 + 32 * li) * 1280;
        const char* wb = sec >= 3 ? (sec == 3 ? wr : wi) + col0 * 1280 : wqkv + (sec * 640 + col0) * 2;
        const unsigned wstep = sec >= 3 ? 128 : 64 * 3840;
        unsigned sb = lds0;
        auto issue = [&]() {
#pragma unroll
            for (int i = 0; i < 4; ++i) dma16(xo[i], xb, sb + li * 4096 + i * 1024);
#pragma unroll
            for (int i = 0; i < 7; ++i) dma16(wo_[i], wb, sb + 16384 + li * 7168 + i * 1024);
            xb += 128; wb += wstep; sb = sb == lds0 + 2 * K1_STAGE ? lds0 : sb + K1_STAGE;
        };
        for (int s = 0; s < 3; ++s) issue();
        WAITV_BAR(22);
        for (int s = 0; s < 7; ++s) { WAITV_BAR(11); issue(); }
        WAITV_BAR(11); WAITV_BAR(0); WAITV_BAR(0);
        return;
    }
    if (sec < 2) k1_compute<0>(ws, lds0, w, lane, rb, sec, grp, col0);
    else if (sec == 2) k1_compute<1>(ws, lds0, w, lane, rb, sec, grp, col0);
    else k1_compute<2>(ws, lds0, w, lane, rb, sec, grp, col0);
}

constexpr unsigned K2_LDS = 73728, G_STAGE = 16384 + 20480;

DI float rowmax16(const f32x16& s) {
    float m = fmaxf(fmaxf(s[0], s[1]), s[2]);
#pragma unroll
    for (int i = 3; i < 15; i += 2) m = fmaxf(fmaxf(m, s[i]), s[i + 1]);
    return fmaxf(m, s[15]);
}
DI float halfmax(float m) { return fmaxf(m, __shfl_xor(m, 32)); }

DI void attn_wg(char* ws, unsigned lds0, int w, int lane, int b) {
    const int head = (b & 7) * 4 + ((b >> 3) & 3), qt = (b >> 5) * 8 + w;
    const unsigned r = lane & 31, hh = lane >> 5;
    const char* kv = ws + WS_KV + head * KV_HS;
    const char* src = kv + (1 + w) * REC;
    const unsigned dst = lds0 + w * REC;
    dma16x3(lane * 16, src, dst);
    const char* qp = ws + WS_Q + (head * 64 + qt) * 1280;
    const u32x4 q1 = *(const u32x4*)(qp + lane * 16);
    u32x2 q2 = *(const u32x2*)(qp + 1024 + r * 8);
    u32x4 k1 = *(const u32x4*)(kv + lane * 16);
    u32x2 k2 = *(const u32x2*)(kv + 1024 + lane * 8);
    const float gate = *(const float*)(ws + WS_G + (qt * 32 + r) * 4);
    if (hh) q2 = (u32x2){0u, 0u};
    const u32x2 q2z = q2;
    f32x16 sa = mma32(k1, q1, mma32k8(k2, q2, f32x16{})), sb;
    dma16x3(lane * 16, src + 8 * REC, dst + 32768);
    const float m0 = halfmax(rowmax16(sa));
    if (hh) q2[0] = pk2(-m0, 0.f);
    sa = mma32(k1, q1, mma32k8(k2, q2, f32x16{}));
    const unsigned vrel = 1536 + hh * 384 + (r < 20 ? r : r < 28 ? 20 : 16) * 16;
    unsigned ak1 = lds0 + lane * 16, ak2 = lds0 + 1024 + lane * 8, av = lds0 + vrel;
    WAITV_BAR(3);
    k1 = lds128(ak1); k2 = lds64(ak2);
    u32x4 v0 = lds128(av), v1 = lds128(av + 768), p0, p1;
    f32x16 o = f32x16{};
    unsigned flip = 0;
#define EX(S, i) S[i] = __builtin_amdgcn_exp2f(S[i])
#define STEP(C, N, i)                                                                                         \
    do {                                                                                                      \
        SBAR();                                                                                               \
        N = mma32k8(k2, q2, f32x16{});                                                                        \
        EX(C, 0); EX(C, 1); EX(C, 2); EX(C, 3); p0[0] = pk2(C[0], C[1]); PIN(C); PIN(p0); SBAR();             \
        N = mma32(k1, q1, N);                                                                                 \
        if (i == 7) {                                                                                         \
            WAITVL_BAR();                                                                                     \
            if (j < 6) dma16x3(lane * 16, src + (j + 2) * 8 * REC, dst + flip);                               \
            flip ^= 32768; ak1 ^= 32768; ak2 ^= 32768; av ^= 32768;                                           \
        }                                                                                                     \
        k2 = lds64(ak2 + ((i + 1) & 7) * REC); k1 = lds128(ak1 + ((i + 1) & 7) * REC);                        \
        EX(C, 4); EX(C, 5); p0[1] = pk2(C[2], C[3]); EX(C, 6); EX(C, 7); p0[2] = pk2(C[4], C[5]);             \
        EX(C, 8); p0[3] = pk2(C[6], C[7]); EX(C, 9); EX(C, 10); PIN(C); PIN(p0); SBAR();                      \
        o = mma32(v0, p0, o);                                                                                 \
        v0 = lds128(av + ((i + 1) & 7) * REC);                                                                \
        EX(C, 11); p1[0] = pk2(C[8], C[9]); EX(C, 12); EX(C, 13); p1[1] = pk2(C[10], C[11]);                  \
        EX(C, 14); EX(C, 15); p1[2] = pk2(C[12], C[13]); p1[3] = pk2(C[14], C[15]); PIN(p1); SBAR();          \
        o = mma32(v1, p1, o);                                                                                 \
        v1 = lds128(av + 768 + ((i + 1) & 7) * REC);                                                          \
    } while (0)
    for (int j = 0; j < 8; ++j) {
        STEP(sa, sb, 0); STEP(sb, sa, 1); STEP(sa, sb, 2); STEP(sb, sa, 3);
        STEP(sa, sb, 4); STEP(sb, sa, 5); STEP(sa, sb, 6); STEP(sb, sa, 7);
    }
#undef STEP
    float l = hh ? o[8] : o[12];
    float chk = l;
#pragma unroll
    for (int i = 0; i < 12; ++i) chk = fmaxf(chk, fabsf(o[i]) * 0x1p-20f) + (o[i] - o[i]);
    if (__builtin_expect(__any(!(chk < 0x1p100f)), 0)) {
        float m = -INFINITY;
        for (int t = 0; t < 64; ++t) {
            const u32x4 a = *(const u32x4*)(kv + t * REC + lane * 16); const u32x2 c = *(const u32x2*)(kv + t * REC + 1024 + lane * 8);
            m = fmaxf(m, rowmax16(mma32(a, q1, mma32k8(c, q2z, f32x16{}))));
        }
        m = halfmax(m);
        o = f32x16{};
        for (int t = 0; t < 64; ++t) {
            const u32x4 a = *(const u32x4*)(kv + t * REC + lane * 16); const u32x2 c = *(const u32x2*)(kv + t * REC + 1024 + lane * 8);
            const u32x4 va = *(const u32x4*)(kv + (t + 1) * REC + vrel), vb = *(const u32x4*)(kv + (t + 1) * REC + 768 + vrel);
            f32x16 s = mma32(a, q1, mma32k8(c, q2z, f32x16{}));
#pragma unroll
            for (int i = 0; i < 16; ++i) s[i] = __builtin_amdgcn_exp2f(s[i] - m);
            o = mma32(va, (u32x4){pk2(s[0], s[1]), pk2(s[2], s[3]), pk2(s[4], s[5]), pk2(s[6], s[7])}, o);
            o = mma32(vb, (u32x4){pk2(s[8], s[9]), pk2(s[10], s[11]), pk2(s[12], s[13]), pk2(s[14], s[15])}, o);
        }
        l = hh ? o[8] : o[12];
    }
#undef EX
    const float sc = (1.f - gate) / l;
    *(u32x4*)(ws + WS_O1 + (qt * 32 + head) * 1024 + lane * 16) =
        (u32x4){pk2(o[0] * sc, o[1] * sc), pk2(o[2] * sc, o[3] * sc), pk2(o[4] * sc, o[5] * sc), pk2(o[6] * sc, o[7] * sc)};
    if (!hh) *(u32x2*)(ws + WS_O2 + (qt * 32 + head) * 256 + r * 8) = (u32x2){pk2(o[8] * sc, o[9] * sc), pk2(o[10] * sc, o[11] * sc)};
}

DI void dft_wg(const char* cr, const char* ci, const char* wo, char* ws, unsigned lds0, int w, int lane, int b) {
    const int xcd = b & 7, idx = b >> 3, kq = xcd >> 1, mt = (xcd & 1) * 8 + (idx >> 2), nt = idx & 3;
    if (w >= 4) {
        const int li = w - 4;
        unsigned co[4];
#pragma unroll
        for (int i = 0; i < 4; ++i) { const unsigned row = 8 * i + (lane >> 3); co[i] = row * 4096 + (((lane & 7) ^ ((row >> 1) & 7)) << 4); }
        const char* cb = (kq < 2 ? cr : ci) + (size_t)(mt * 128 + 32 * li) * 4096 + (kq & 1) * 2048;
        const char* bb = ws + WS_AB + (size_t)((((kq >> 1) * 4 + nt) * 128 + (kq & 1) * 64 + li) * 5) * 1024;
        unsigned sb = lds0;
        auto issue = [&]() {
#pragma unroll
            for (int i = 0; i < 4; ++i) dma16(co[i], cb, sb + li * 4096 + i * 1024);
            dma16x3(lane * 16, bb, sb + 16384 + li * 5120);
            dma16(lane * 16, bb + 3072, sb + 16384 + li * 5120 + 3072);
            dma16(lane * 16, bb + 4096, sb + 16384 + li * 5120 + 4096);
            cb += 128; bb += 20480; sb = sb == lds0 ? lds0 + G_STAGE : lds0;
        };
        issue(); issue();
        WAITV_BAR(9);
        for (int s = 0; s < 14; ++s) { WAITV_BAR(0); issue(); }
        WAITV_BAR(0); WAITV_BAR(0);
        const int f = b * 4 + li;
        if (f < 800) {
            const int cbk = f / 100, st = (f - cbk * 100) / 5, tile = f % 5, kh = st / 10, i = st - kh * 10;
            const int rr = lane & 15, g = lane >> 4;
            const int e = cbk * 80 + (tile < 4 ? 32 * (tile >> 1) + 8 * (rr >> 2) + 4 * (tile & 1) + (rr & 3) : 64 + rr);
            const int k0 = i < 8 ? (16 * kh + 2 * i + (g >> 1)) * 20 + 8 * (g & 1) : (16 * kh + 8 * (i - 8) + 2 * g) * 20 + 16, k4 = i < 8 ? k0 + 4 : k0 + 20;
            const unsigned short* p = (const unsigned short*)wo + e;
            u32x4 v;
            v[0] = p[k0 * 640] | ((unsigned)p[(k0 + 1) * 640] << 16); v[1] = p[(k0 + 2) * 640] | ((unsigned)p[(k0 + 3) * 640] << 16);
            v[2] = p[k4 * 640] | ((unsigned)p[(k4 + 1) * 640] << 16); v[3] = p[(k4 + 2) * 640] | ((unsigned)p[(k4 + 3) * 640] << 16);
            *(u32x4*)(ws + WS_WT + f * 1024 + lane * 16) = v;
        }
        return;
    }
    const unsigned r = lane & 31, hh = lane >> 5;
    unsigned ac = lds0 + w * 4096 + r * 128 + ((hh ^ ((r >> 1) & 7)) << 4), ab = lds0 + 16384 + lane * 16;
    f32x16 acc[5];
#pragma unroll
    for (int j = 0; j < 5; ++j) acc[j] = f32x16{};
    u32x4 cf[2], bf[5];
    WAITL_BAR();
    cf[0] = lds128(ac);
#pragma unroll
    for (int j = 0; j < 5; ++j) bf[j] = lds128(ab + j * 1024);
    int dlt = G_STAGE;
    for (int s = 0; s < 16; ++s) {
#pragma unroll
        for (int ks = 0; ks < 4; ++ks) {
            const int c = ks & 1, nks = (ks + 1) & 3;
            if (ks == 3) { WAITL_BAR(); ac += dlt; ab += dlt; dlt = -dlt; }
            SBAR();
            cf[c ^ 1] = lds128(ac ^ (nks << 5));
#pragma unroll
            for (int j = 0; j < 5; ++j) { acc[j] = mma32(bf[j], cf[c], acc[j]); bf[j] = lds128(ab + nks * 5120 + j * 1024); SBAR(); }
        }
    }
    char* p = ws + WS_P + (size_t)((kq * 64 + mt * 4 + w) * 80 + nt * 20) * 512 + lane * 16;
#pragma unroll
    for (int j = 0; j < 5; ++j)
#pragma unroll
        for (int s = 0; s < 2; ++s) {
            const f32x16& a = acc[j];
            *(u32x4*)(p + (4 * j + 2 * s) * 512) = (u32x4){pk2(a[8 * s], a[8 * s + 1]), pk2(a[8 * s + 2], a[8 * s + 3]), pk2(a[8 * s + 4], a[8 * s + 5]), pk2(a[8 * s + 6], a[8 * s + 7])};
        }
}

__global__ __launch_bounds__(512, 4) void k2_mix(const char* cr, const char* ci, const char* wo, char* ws) {
    extern __shared__ __attribute__((aligned(16))) char smem[];
    const unsigned lds0 = (unsigned)(uintptr_t)smem;
    const int lane = threadIdx.x & 63, w = __builtin_amdgcn_readfirstlane(threadIdx.x >> 6);
    if (blockIdx.x < 256) attn_wg(ws, lds0, w, lane, blockIdx.x);
    else dft_wg(cr, ci, wo, ws, lds0, w, lane, blockIdx.x - 256);
}

constexpr unsigned K3_LDS = 102400 + 8 * 3072;

__global__ __launch_bounds__(512, 2) void k3_out(const char* ws, char* out) {
    extern __shared__ __attribute__((aligned(16))) char smem[];
    const unsigned lds0 = (unsigned)(uintptr_t)smem;
    const int lane = threadIdx.x & 63, w = __builtin_amdgcn_readfirstlane(threadIdx.x >> 6);
    const int b = blockIdx.x, xcd = b & 7, idx = b >> 3, rbk = (xcd >> 1) * 8 + (idx >> 2), cb = (xcd & 1) * 4 + (idx & 3);
    const int ts = w & 3, kh = w >> 2, g = lane >> 4, tt = rbk * 2 + (ts >> 1), tq = (ts & 1) * 16 + (lane & 15);
    for (int p = w; p < 100; p += 8) dma16(lane * 16, ws + WS_WT + (cb * 100 + p) * 1024, lds0 + p * 1024);
    u32x4 of[10];
    const char* o1 = ws + WS_O1 + (tt * 32 + 16 * kh) * 1024 + g * 512 + tq * 16;
    const char* o2 = ws + WS_O2 + (tt * 32 + 16 * kh) * 256 + g * 512 + tq * 8;
#pragma unroll
    for (int i = 0; i < 8; ++i) of[i] = *(const u32x4*)(o1 + i * 2048);
#pragma unroll
    for (int i = 0; i < 2; ++i) { const u32x2 lo = *(const u32x2*)(o2 + i * 2048), hi = *(const u32x2*)(o2 + i * 2048 + 256); of[8 + i] = (u32x4){lo[0], lo[1], hi[0], hi[1]}; }
    u32x4 pa[4]; u32x2 pb[4];
    const char* pp = ws + WS_P + (tt * 80 + cb * 10) * 512 + tq * 16;
#pragma unroll
    for (int q = 0; q < 4; ++q) { pa[q] = *(const u32x4*)(pp + q * (64 * 80 * 512) + (4 * kh + g) * 512); pb[q] = *(const u32x2*)(pp + q * (64 * 80 * 512) + (8 + (g >> 1)) * 512 + (g & 1) * 8); }
    const float gate = *(const float*)(ws + WS_G + (tt * 32 + tq) * 4);
    f32x4 acc[5];
#pragma unroll
    for (int j = 0; j < 5; ++j) acc[j] = f32x4{};
    WAITV_BAR(0);
    const unsigned aw = lds0 + kh * 51200 + lane * 16;
#pragma unroll
    for (int i = 0; i < 10; ++i)
#pragma unroll
        for (int j = 0; j < 5; ++j) acc[j] = mma16(lds128(aw + (i * 5 + j) * 1024), of[i], acc[j]);
    LAS f32x4* mine = (LAS f32x4*)(uintptr_t)(lds0 + 102400 + w * 3072 + lane * 16);
    const LAS f32x4* other = (const LAS f32x4*)(uintptr_t)(lds0 + 102400 + (w ^ 4) * 3072 + lane * 16);
    mine[0] = kh ? acc[0] : acc[2]; mine[64] = kh ? acc[1] : acc[3]; mine[128] = acc[4];
    WAITL_BAR();
    f32x4 a0 = (kh ? acc[2] : acc[0]) + other[0], a1 = (kh ? acc[3] : acc[1]) + other[64], a2 = acc[4] + other[128];
    float y[12];
#pragma unroll
    for (int e = 0; e < 4; ++e) { y[2 * e] = (bflo(pa[0][e]) + bflo(pa[1][e])) + (bflo(pa[2][e]) + bflo(pa[3][e])); y[2 * e + 1] = (bfhi(pa[0][e]) + bfhi(pa[1][e])) + (bfhi(pa[2][e]) + bfhi(pa[3][e])); }
#pragma unroll
    for (int e = 0; e < 2; ++e) { y[8 + 2 * e] = (bflo(pb[0][e]) + bflo(pb[1][e])) + (bflo(pb[2][e]) + bflo(pb[3][e])); y[9 + 2 * e] = (bfhi(pb[0][e]) + bfhi(pb[1][e])) + (bfhi(pb[2][e]) + bfhi(pb[3][e])); }
    char* op = out + (tt * 32 + tq) * 1280 + cb * 160;
    *(u32x4*)(op + kh * 64 + g * 16) = (u32x4){pk2(gate * y[0] + a0[0], gate * y[1] + a0[1]), pk2(gate * y[2] + a0[2], gate * y[3] + a0[3]),
                                               pk2(gate * y[4] + a1[0], gate * y[5] + a1[1]), pk2(gate * y[6] + a1[2], gate * y[7] + a1[3])};
    if (!kh) *(u32x2*)(op + 128 + g * 8) = (u32x2){pk2(gate * y[8] + a2[0], gate * y[9] + a2[1]), pk2(gate * y[10] + a2[2], gate * y[11] + a2[3])};
}

extern "C" void kernel_launch(void* const* d_in, const int* in_sizes, int n_in, void* d_out, int out_size, void* d_ws, size_t ws_size, hipStream_t stream) {
    const char* x = (const char*)d_in[0];
    const char* cr = (const char*)d_in[1];
    const char* ci = (const char*)d_in[2];
    const char* wr = (const char*)d_in[3];
    const char* wi = (const char*)d_in[4];
    const char* wqkv = (const char*)d_in[6];
    const char* wo = (const char*)d_in[7];
    const char* gw = (const char*)d_in[8];
    char* ws = (char*)d_ws;
    (void)hipFuncSetAttribute((const void*)k1_proj, hipFuncAttributeMaxDynamicSharedMemorySize, K1_LDS);
    (void)hipFuncSetAttribute((const void*)k2_mix, hipFuncAttributeMaxDynamicSharedMemorySize, K2_LDS);
    (void)hipFuncSetAttribute((const void*)k3_out, hipFuncAttributeMaxDynamicSharedMemorySize, K3_LDS);
    k1_proj<<<256, 512, K1_LDS, stream>>>(x, wr, wi, wqkv, gw, ws);
    k2_mix<<<512, 512, K2_LDS, stream>>>(cr, ci, wo, ws);
    k3_out<<<256, 512, K3_LDS, stream>>>(ws, (char*)d_out);
}
```

```cpp
#include <hip/hip_runtime.h>
#include <hip/hip_bf16.h>
#include <stdint.h>

typedef __attribute__((ext_vector_type(8))) __bf16 bf16x8;
typedef __attribute__((ext_vector_type(2))) __bf16 bf16x2;
typedef __attribute__((ext_vector_type(4))) short s16x4;
typedef __attribute__((ext_vector_type(16))) float f32x16;
typedef __attribute__((ext_vector_type(4))) float f32x4;
typedef __attribute__((ext_vector_type(2))) float f32x2;
typedef __attribute__((ext_vector_type(4))) unsigned u32x4;
typedef __attribute__((ext_vector_type(2))) unsigned u32x2;
typedef unsigned long long u64;
typedef __amdgpu_buffer_rsrc_t rsrc_t;
#define LAS __attribute__((address_space(3)))
#define GAS __attribute__((address_space(1)))
#define DI __device__ __forceinline__
#define SBAR() __builtin_amdgcn_sched_barrier(0)
#define PIN(x) asm volatile("" : "+v"(x))
#define PINS(x) asm volatile("" : "+s"(x))
#define PRIO(n) __builtin_amdgcn_s_setprio(n)

constexpr unsigned REC = 3072, KV_HS = 65 * REC, QT = 1536, Q_HS = 64 * QT;
constexpr unsigned WS_KV = 0;
constexpr unsigned WS_Q = WS_KV + 32 * KV_HS;
constexpr unsigned WS_AB = WS_Q + 32 * Q_HS;
constexpr unsigned WS_P = WS_AB + 8 * 64 * 10240;
constexpr unsigned WS_O1 = WS_P + 4 * 256 * 10240;
constexpr unsigned WS_O2 = WS_O1 + 64 * 32 * 1024;
constexpr unsigned WS_WT = WS_O2 + 64 * 32 * 256;
constexpr unsigned WS_G = WS_WT + 800 * 1024;
constexpr unsigned WS_F = WS_G + 8192;
constexpr unsigned WS_E = WS_F + 6 * 256 * 128;
constexpr unsigned WS_END = WS_E + 128;
constexpr unsigned FQ = 0, FA = 256, FP = 512, FW = 768, FL = 1024, FO = 1280;
constexpr unsigned LDS_BYTES = 81920, CODE_BYTES = 24448;
constexpr float QSCALE = 0.22360679774997896f * 1.4426950408889634f;
#define PACE 3
#define AAT 6
#define NL 8
#define NA 8
#define NO 1
#define GSW 2

DI unsigned swap23(unsigned r) { return (r & ~12u) | ((r & 4u) << 1) | ((r & 8u) >> 1); }
DI unsigned pk2(float a, float b) { f32x2 v = {a, b}; return __builtin_bit_cast(unsigned, __builtin_convertvector(v, bf16x2)); }
DI float bflo(unsigned u) { return __uint_as_float(u << 16); }
DI float bfhi(unsigned u) { return __uint_as_float(u & 0xffff0000u); }
DI f32x16 mma32(u32x4 a, u32x4 b, f32x16 c) { return __builtin_amdgcn_mfma_f32_32x32x16_bf16(__builtin_bit_cast(bf16x8, a), __builtin_bit_cast(bf16x8, b), c, 0, 0, 0); }
DI f32x16 mma32k8(u32x2 a, u32x2 b, f32x16 c) { return __builtin_amdgcn_mfma_f32_32x32x8bf16_1k(__builtin_bit_cast(s16x4, a), __builtin_bit_cast(s16x4, b), c, 0, 0, 0); }
DI f32x4 mma16(u32x4 a, u32x4 b, f32x4 c) { return __builtin_amdgcn_mfma_f32_16x16x32_bf16(__builtin_bit_cast(bf16x8, a), __builtin_bit_cast(bf16x8, b), c, 0, 0, 0); }
DI u32x4 pk8(const f32x16& a, int s, float c) { return (u32x4){pk2(a[8 * s] * c, a[8 * s + 1] * c), pk2(a[8 * s + 2] * c, a[8 * s + 3] * c), pk2(a[8 * s + 4] * c, a[8 * s + 5] * c), pk2(a[8 * s + 6] * c, a[8 * s + 7] * c)}; }
DI u32x4 pk8(const f32x16& a, int s) { return (u32x4){pk2(a[8 * s], a[8 * s + 1]), pk2(a[8 * s + 2], a[8 * s + 3]), pk2(a[8 * s + 4], a[8 * s + 5]), pk2(a[8 * s + 6], a[8 * s + 7])}; }
DI u32x4 pk44(f32x4 a, f32x4 b) { return (u32x4){pk2(a[0], a[1]), pk2(a[2], a[3]), pk2(b[0], b[1]), pk2(b[2], b[3])}; }
DI f32x16 zero16() { u32x2 z = {0u, 0u}; PIN(z); return mma32k8(z, z, f32x16{}); }
DI u32x4 lds128(unsigned a) { return *(const LAS u32x4*)(uintptr_t)a; }
DI u32x2 lds64(unsigned a) { return *(const LAS u32x2*)(uintptr_t)a; }
DI u32x2 ldstr(unsigned a) { return __builtin_bit_cast(u32x2, __builtin_amdgcn_ds_read_tr16_b64_v4i16((LAS s16x4*)(uintptr_t)a)); }
DI void st128(rsrc_t rs, unsigned v, unsigned s, u32x4 d) { __builtin_amdgcn_raw_buffer_store_b128(d, rs, v, s, 16); asm volatile("s_nop 1" :: "v"(d) : "memory"); }
DI void st64(rsrc_t rs, unsigned v, unsigned s, u32x2 d) { __builtin_amdgcn_raw_buffer_store_b64(d, rs, v, s, 16); }
DI void st32(rsrc_t rs, unsigned v, unsigned s, unsigned d) { __builtin_amdgcn_raw_buffer_store_b32(d, rs, v, s, 16); }
DI u32x4 ld128(rsrc_t rs, unsigned v, unsigned s) { return __builtin_amdgcn_raw_buffer_load_b128(rs, v, s, 16); }
DI u32x2 ld64(rsrc_t rs, unsigned v, unsigned s) { return __builtin_amdgcn_raw_buffer_load_b64(rs, v, s, 16); }
DI unsigned ld32(rsrc_t rs, unsigned v, unsigned s) { return __builtin_amdgcn_raw_buffer_load_b32(rs, v, s, 16); }
#define M0SET "s_mov_b32 m0, %[l]\n\ts_nop 0\n\t"
#define GLD(v, o, c) "global_load_lds_dwordx4 %[" v "], %[b] offset:" o c "\n\t"
DI void dma1(unsigned v0, const char* base, unsigned lds) { asm volatile(M0SET GLD("v0", "0", "") ::[v0] "v"(v0), [b] "s"(base), [l] "s"(lds) : "memory"); }
DI void dma2(unsigned v0, unsigned v1, const char* base, unsigned lds) { asm volatile(M0SET GLD("v0", "0", "") GLD("v1", "1024", "") ::[v0] "v"(v0), [v1] "v"(v1), [b] "s"(base), [l] "s"(lds) : "memory"); }
DI void dma4(unsigned v0, unsigned v1, unsigned v2, unsigned v3, const char* base, unsigned lds) {
    asm volatile(M0SET GLD("v0", "0", "") GLD("v1", "1024", "") GLD("v2", "2048", "") GLD("v3", "3072", "") ::[v0] "v"(v0), [v1] "v"(v1), [v2] "v"(v2), [v3] "v"(v3), [b] "s"(base), [l] "s"(lds) : "memory");
}
DI void dma1c(unsigned v0, const char* base, unsigned lds) { asm volatile(M0SET GLD("v0", "0", " sc1") ::[v0] "v"(v0), [b] "s"(base), [l] "s"(lds) : "memory"); }
DI void dma4c(unsigned v0, const char* base, unsigned lds) {
    asm volatile(M0SET GLD("v0", "0", " sc1") GLD("v0", "1024", " sc1") GLD("v0", "2048", " sc1") GLD("v0", "3072", " sc1") ::[v0] "v"(v0), [b] "s"(base), [l] "s"(lds) : "memory");
}
DI void dma5c(unsigned v0, const char* base, unsigned lds) { dma4c(v0, base, lds); dma1c(v0, base + 4096, lds + 4096); }
DI void dma_rec(unsigned v0, unsigned v1, const char* base, unsigned lds) {
    asm volatile(M0SET GLD("v0", "0", " sc1") "s_mov_b32 exec_hi, 0xff0fffff\n\t" GLD("v1", "1024", " sc1") "s_mov_b32 exec_lo, 0xffff0fff\n\ts_mov_b32 exec_hi, 0x0fffff0f\n\t" GLD("v0", "2048", " sc1") "s_mov_b64 exec, -1"
                 ::[v0] "v"(v0), [v1] "v"(v1), [b] "s"(base), [l] "s"(lds) : "memory");
}
#define BAR() asm volatile("s_barrier" ::: "memory")
#define WAITV_BAR(N) asm volatile("s_waitcnt vmcnt(" #N ")\n\ts_barrier" ::: "memory")
#define WAITL_BAR() asm volatile("s_waitcnt lgkmcnt(0)\n\ts_barrier" ::: "memory")
#define WAITVL_BAR() asm volatile("s_waitcnt vmcnt(0) lgkmcnt(0)\n\ts_barrier" ::: "memory")

DI u64 ldflag(const char* ws, unsigned off) { return __hip_atomic_load((const GAS u64*)(ws + off), __ATOMIC_RELAXED, __HIP_MEMORY_SCOPE_AGENT); }
DI void stflag(char* ws, unsigned off, u64 v) { __hip_atomic_store((GAS u64*)(ws + off), v, __ATOMIC_RELAXED, __HIP_MEMORY_SCOPE_AGENT); }
DI u64 mktag(u64 e) {
    const u64 u = ((u64)(unsigned)__builtin_amdgcn_readfirstlane((unsigned)(e >> 32)) << 32) | (unsigned)__builtin_amdgcn_readfirstlane((unsigned)e);
    return u * 6364136223846793005ull + 1442695040888963407ull;
}
struct Flag {
    unsigned v; u32x2 d;
    DI Flag(bool mine, unsigned f, u64 tag) { v = mine ? WS_F + f * 128 : WS_END; d = (u32x2){(unsigned)tag, (unsigned)(tag >> 32)}; PIN(v); PIN(d); }
    DI void raise(rsrc_t rs) const { st64(rs, v, 0, d); }
};
template <int NAP = 1> DI void await(const char* ws, unsigned f0, u64 tag) {
    u64 a; unsigned n = 1u << 19;
    asm volatile("1:\n\tglobal_load_dwordx2 %0, %2, %3 sc1\n\ts_sub_u32 %1, %1, 1\n\ts_sleep %c5\n\ts_waitcnt vmcnt(0)\n\tv_cmp_ne_u64 vcc, %4, %0\n\ts_cbranch_scc1 2f\n\ts_cbranch_vccnz 1b\n2:"
                 : "=&v"(a), "+s"(n) : "v"(WS_F + f0 * 128), "s"(ws), "s"(tag), "i"(NAP) : "vcc", "scc", "memory");
}
DI void await(const char* ws, unsigned f0, unsigned f1, u64 tag) {
    u64 a, b, m; unsigned n = 1u << 19;
    asm volatile("1:\n\tglobal_load_dwordx2 %0, %4, %6 sc1\n\tglobal_load_dwordx2 %1, %5, %6 sc1\n\ts_sub_u32 %3, %3, 1\n\ts_cbranch_scc1 2f\n\ts_waitcnt vmcnt(0)\n\t"
                 "v_cmp_ne_u64 %2, %7, %0\n\tv_cmp_ne_u64 vcc, %7, %1\n\ts_or_b64 vcc, vcc, %2\n\ts_cbranch_scc1 1b\n2:\n\ts_waitcnt vmcnt(0)"
                 : "=&v"(a), "=&v"(b), "=&s"(m), "+s"(n) : "v"(WS_F + f0 * 128), "v"(WS_F + f1 * 128), "s"(ws), "s"(tag) : "vcc", "scc", "memory");
}

template <int T>
DI void qkv_compute(rsrc_t rs, int w, int lane, int rb, int cg, int b, u64 ep) {
    const unsigned r = lane & 31, hh = lane >> 5, wm = w & 1, wn = w >> 1, q = (lane & 15) >> 2, p = lane & 3;
    const unsigned pp = T == 0 ? ((p & 1) << 1) | (p >> 1) : p;
    unsigned xa = (64 * wm + r) * 128 + ((hh ^ ((r >> 1) & 7)) << 4), wa[2];
    wa[0] = 49152 + (8 * hh + q) * 256 + (((2 * wn) ^ q) << 6) + ((lane >> 4) & 1) * 32 + pp * 8; wa[1] = wa[0] ^ 64;
    PIN(xa); PIN(wa[0]); PIN(wa[1]);
    const unsigned kt = rb * 4 + 2 * wm;
    const bool isk = cg >= 5;
    unsigned va[4], sb, ts = REC;
    if (T == 0) {
        const unsigned ct = isk ? cg - 5 : cg, hs = isk ? KV_HS : Q_HS, slot = isk ? r : swap23(r);
        ts = isk ? REC : QT; sb = (isk ? WS_KV : WS_Q) + kt * ts;
#pragma unroll
        for (int i = 0; i < 4; ++i) { const unsigned oc = ct * 16 + 8 * wn + 2 * i + hh, hp = oc / 5, ty = oc - hp * 5; va[i] = (2 * hp + (ty > 2)) * hs + (ty > 2 ? ty - 3 : ty) * 512 + slot * 16; PIN(va[i]); }
    } else {
        sb = WS_KV + kt * REC + 1536;
#pragma unroll
        for (int i = 0; i < 2; ++i) { const unsigned c = (cg - 10) * 128 + 64 * wn + 32 * i + r, head = c / 20, d = c - head * 20; va[i] = head * KV_HS + hh * 384 + (d < 16 ? swap23(d) : d) * 16; PIN(va[i]); }
    }
    f32x16 acc[2][2];
#pragma unroll
    for (int i = 0; i < 4; ++i) { acc[i >> 1][i & 1] = zero16(); PIN(acc[i >> 1][i & 1]); }
    u32x4 xf[2][2], wf[2][2];
    auto rd_x = [&](int set, int g, int mi) { xf[set][mi] = lds128((xa ^ ((g & 3) << 5)) + mi * 4096); };
    auto rd_w = [&](int set, int g, int ni) {
        const unsigned a = wa[ni] + (g & 7) * 4096;
        const u32x2 lo = ldstr(a), hi = ldstr(a + 1024); wf[set][ni] = (u32x4){lo[0], lo[1], hi[0], hi[1]};
    };
    WAITL_BAR();
    rd_x(0, 0, 0); rd_w(0, 0, 0); rd_x(0, 0, 1); rd_w(0, 0, 1);
    unsigned xs = 0;
#pragma unroll 1
    for (int t = 0; t < 5; ++t) {
        if (__builtin_expect(t == PACE && w == 0, 0)) Flag(lane == 0, FL + b, mktag(ep)).raise(rs);
#pragma unroll
        for (int g = 0; g < 8; ++g) {
            const int c = g & 1, n = c ^ 1;
            if (c) WAITL_BAR();
            if ((g & 3) == 3) { const unsigned d = xs == 32768 ? -32768 : 16384; xs += d; xa += d; }
            SBAR();
#pragma unroll
            for (int i = 0; i < 4; ++i) {
                const int mi = i & 1, ni = i >> 1;
                acc[mi][ni] = T == 0 ? mma32(wf[c][ni], xf[c][mi], acc[mi][ni]) : mma32(xf[c][mi], wf[c][ni], acc[mi][ni]);
                if (i == 0) { rd_x(n, g + 1, 0); rd_w(n, g + 1, 0); }
                if (i == 1) { rd_x(n, g + 1, 1); rd_w(n, g + 1, 1); }
                SBAR();
            }
        }
    }
    const float sc = isk ? 1.f : QSCALE;
#pragma unroll
    for (int i = 0; i < 8; ++i) {
        const int mi = (i >> 1) & 1, ni = i >> 2, s = i & 1;
        if (T == 0) st128(rs, va[2 * ni + s], sb + mi * ts, pk8(acc[mi][ni], s, sc));
        else st128(rs, va[ni], sb + (mi + s) * REC + s * 768, pk8(acc[mi][ni], s));
    }
}

DI void qkv_load(const char* x, const char* wqkv, int li, int lane, int rb, int cg) {
    const int rot = 5 * ((rb ^ (cg >> 2)) & 1);
    if (li >= 2) {
        unsigned v0 = (lane >> 4) * 3840 + ((((lane >> 2) & 3) ^ (lane >> 4)) << 6) + (lane & 3) * 16, v1 = v0 + 14336, v2 = v0 + 2 * 14336, v3 = v0 + 3 * 14336;
        unsigned off = cg * 256 + (16 * (li - 2) + 64 * rot) * 3840, lds = 49152 + (li - 2) * 4096;
        PIN(v0); PIN(v1); PIN(v2); PIN(v3); PINS(off); PINS(lds); SBAR();
        const char* base = wqkv + off;
        unsigned slot = 0; int left = 20 - 2 * rot;
        auto issue = [&]() { dma4(v0, v1, v2, v3, base, lds + slot); base += --left ? 32 * 3840 : (32 - 640) * 3840; slot = (slot + 8192) & 32767; };
        issue(); issue();
        WAITV_BAR(4);
        issue();
#pragma unroll 1
        for (int s = 0; s < 17; ++s) { issue(); WAITV_BAR(8); }
        WAITV_BAR(4); WAITV_BAR(0); BAR();
    } else {
        const unsigned row = lane >> 3;
        unsigned v0 = swap23(row) * 1280 + (((lane & 7) ^ (row >> 1)) << 4), v1 = (v0 ^ 64) + 4 * 1280 - 1024;
        unsigned off = (rb * 128 + 64 * li) * 1280 + rot * 128, lds = li * 8192;
        PIN(v0); PIN(v1); PINS(off); PINS(lds); SBAR();
        const char* base = x + off;
        int left = 10 - rot;
        auto issue = [&]() {
#pragma unroll
            for (int i = 0; i < 4; ++i) dma2(v0, v1, base + i * 16 * 1280, lds + i * 2048);
            base += --left ? 128 : 128 - 1280; lds = lds >= 32768 ? lds - 32768 : lds + 16384;
        };
#pragma unroll 1
        for (int s = 0; s < 2; ++s) issue();
        WAITV_BAR(8);
#pragma unroll 1
        for (int s = 0; s < 8; ++s) { issue(); BAR(); WAITV_BAR(8); }
        BAR(); WAITV_BAR(0); BAR(); BAR();
    }
}

DI void gate_rows(const char* x, const char* gw, rsrc_t rs, int rb) {
    const int row = rb * 128 + (threadIdx.x >> 2), sub = threadIdx.x & 3;
    float s = 0.f;
#pragma unroll 5
    for (int i = 0; i < 20; ++i) {
        const u32x4 a = *(const u32x4*)(x + row * 1280 + (4 * i + sub) * 16), g = *(const u32x4*)(gw + (4 * i + sub) * 16);
#pragma unroll
        for (int e = 0; e < 4; ++e) s += bflo(a[e]) * bflo(g[e]) + bfhi(a[e]) * bfhi(g[e]);
    }
    s += __shfl_xor(s, 1); s += __shfl_xor(s, 2);
    if (sub == 0) st32(rs, row * 4, WS_G, __float_as_uint(1.f / (1.f + __expf(-s))));
}

DI float rowmax16(const f32x16& s) {
    float m = fmaxf(fmaxf(s[0], s[1]), s[2]);
#pragma unroll
    for (int i = 3; i < 15; i += 2) m = fmaxf(fmaxf(m, s[i]), s[i + 1]);
    return fmaxf(m, s[15]);
}
DI float halfmax(float m) { const auto t = __builtin_amdgcn_permlane32_swap(__float_as_uint(m), __float_as_uint(m), false, false); return fmaxf(__uint_as_float(t[0]), __uint_as_float(t[1])); }
DI float upper(float m) { const auto t = __builtin_amdgcn_permlane32_swap(__float_as_uint(m), __float_as_uint(m), false, false); return __uint_as_float(t[1]); }

DI void attention(char* ws, rsrc_t rs, int w, int lane, int head, int qb, u64 tag) {
    const unsigned r = lane & 31, hh = lane >> 5, kh = w >> 2, qt = qb * 8 + 2 * (w & 3), odd = head & 1;
    const unsigned kv = WS_KV + head * KV_HS, kv2 = kv - odd * KV_HS + 1024;
    unsigned l16 = lane * 16, l8 = hh ? WS_END : r * 16 + odd * 8, d0 = l16 + KV_HS, d1 = hh ? d0 : d0 - odd * KV_HS;
    unsigned vg = r < 20 ? 1536 + hh * 384 + r * 16 : WS_END, vfill = r == 20 ? 0x3f803f80u : 0u, kone = hh ? 0x3f80u : 0u;
    const char* src = ws + kv + (32 * kh + 1 + (w & 3)) * REC - KV_HS;
    unsigned dst = w * REC, kvh = kv + 32 * kh * REC, kv2h = kv2 + 32 * kh * REC;
    unsigned qa = WS_Q + head * Q_HS + (qt + 1 - kh) * QT, qa2 = qa + 1024 - odd * Q_HS;
    unsigned o1 = WS_O1 + ((qt + kh) * 32 + head) * 1024, o2 = WS_O2 + ((qt + kh) * 32 + head) * 256;
    const unsigned lb = kh * 4 * REC;
    unsigned ak1 = lb + l16, ak2 = lb + (hh ? 1536 + 22 * 16 : 1024 + r * 16 + odd * 8), av = lb + 1536 + (hh ^ (r > 27)) * 384 + min(r, 21u) * 16;
    unsigned xm = 57344 + w * 3072 + l16, xo = 57344 + (w ^ 4) * 3072 + l16;
    u32x4 p0, p1 = u32x4{}, v1 = u32x4{};
    f32x16 oa = zero16(), ob = zero16();
    float e[16]; e[12] = 0.f; e[13] = 0.f; e[14] = 0.f; e[15] = 0.f;
    PIN(l16); PIN(l8); PIN(d0); PIN(d1); PIN(vg); PIN(vfill); PIN(kone); PIN(ak1); PIN(ak2); PIN(av); PIN(xm); PIN(xo);
    PIN(p1); PIN(v1); PIN(oa); PIN(ob); PIN(e[12]); PIN(e[13]); PIN(e[14]); PIN(e[15]);
    PINS(src); PINS(dst); PINS(kvh); PINS(kv2h); PINS(qa); PINS(qa2); PINS(o1); PINS(o2);
    if (w == 4) {
        const unsigned c0 = (20 * head) >> 7, c1 = (20 * head + 19) >> 7;
        await(ws, FQ + ((lane >> 1) & 15) * 16 + 5 + 5 * (lane >> 5) + ((lane & 1) ? c1 : c0), FQ + (2 * qb + (lane >> 5)) * 16 + ((lane & 16) ? c1 : c0), tag);
    }
    BAR();
    u32x2 k2 = ld64(rs, l8, kv2h), q2a = ld64(rs, l8, qa2);
    u32x4 k1 = ld128(rs, l16, kvh);
    const u32x4 q1a = ld128(rs, l16, qa);
    SBAR();
    u32x4 v0 = ld128(rs, vg, kvh);
    dma_rec(d0, d1, src, dst);
    SBAR();
    f32x16 sa = mma32(k1, q1a, mma32k8(k2, q2a, f32x16{})), sb;
    *(LAS u32x4*)(uintptr_t)(xm + 1024) = q1a;
    unsigned na = pk2(-halfmax(rowmax16(sa)), 0.f); PIN(na);
    q2a[0] = hh ? na : q2a[0]; k2[0] |= kone;
    *(LAS u32x2*)(uintptr_t)xm = q2a;
    sa = mma32(k1, q1a, mma32k8(k2, q2a, f32x16{}));
    SBAR();
    WAITL_BAR();
    u32x2 q2b = lds64(xo);
    const u32x4 q1b = lds128(xo + 1024);
    v0 |= vfill;
    const int jl = kh ? 7 : 0;
#define EX(C, i) do { e[i] = __builtin_amdgcn_exp2f(C[i]); PIN(e[i]); SBAR(); } while (0)
#define CV(P, n, i) do { unsigned t_ = pk2(e[i], e[i + 1]); PIN(t_); P[n] = t_; SBAR(); } while (0)
#define UNIT(C, N, O, OP, Q1, Q2, u)                                                                          \
    do {                                                                                                      \
        SBAR();                                                                                               \
        N = mma32k8(k2, Q2, f32x16{}); SBAR();                                                                \
        if (u == AAT) PRIO(0);                                                                                \
        if (u == 0) {                                                                                         \
            WAITVL_BAR(); PRIO(2);                                                                            \
            if (__builtin_expect(j + jj < jl, 0)) { src += 4 * REC; dma_rec(d0, d1, src - 32 * REC, dst - 4 * REC + 32768 - fl); dma_rec(d0, d1, src, dst + 32768 - fl); } \
        }                                                                                                     \
        if (!(u & 1)) { k2 = lds64(ak2 + (u >> 1) * REC + fl); SBAR(); }                                      \
        EX(C, 0); CV(p1, 2, 12); EX(C, 1); CV(p1, 3, 14); EX(C, 2); EX(C, 3);                                 \
        OP = mma32(v1, p1, OP); SBAR();                                                                       \
        if (!(u & 1)) { v1 = lds128(av + 768 + (u >> 1) * REC + fl); SBAR(); }                                \
        EX(C, 4); CV(p0, 0, 0); EX(C, 5); CV(p0, 1, 2); EX(C, 6); EX(C, 7);                                   \
        N = mma32(k1, Q1, N); SBAR();                                                                         \
        if (!(u & 1)) { k1 = lds128(ak1 + (u >> 1) * REC + fl); SBAR(); }                                     \
        EX(C, 8); CV(p0, 2, 4); EX(C, 9); CV(p0, 3, 6); EX(C, 10); EX(C, 11);                                 \
        O = mma32(v0, p0, O); SBAR();                                                                         \
        if (u & 1) { v0 = lds128(av + (u >> 1) * REC + fl); SBAR(); }                                         \
        EX(C, 12); CV(p1, 0, 8); EX(C, 13); CV(p1, 1, 10); EX(C, 14); EX(C, 15);                              \
    } while (0)
#pragma unroll 1
    for (int j = 0; j < 8; j += 2) {
#pragma unroll
        for (int jj = 0; jj < 2; ++jj) {
            const unsigned fl = jj * 32768;
            UNIT(sa, sb, oa, ob, q1b, q2b, 0); UNIT(sb, sa, ob, oa, q1a, q2a, 1); UNIT(sa, sb, oa, ob, q1b, q2b, 2); UNIT(sb, sa, ob, oa, q1a, q2a, 3);
            UNIT(sa, sb, oa, ob, q1b, q2b, 4); UNIT(sb, sa, ob, oa, q1a, q2a, 5); UNIT(sa, sb, oa, ob, q1b, q2b, 6); UNIT(sb, sa, ob, oa, q1a, q2a, 7);
        }
    }
#undef UNIT
#undef EX
#undef CV
    LAS f32x4* mine = (LAS f32x4*)(uintptr_t)xm;
    const LAS f32x4* other = (const LAS f32x4*)(uintptr_t)xo;
    p1[2] = pk2(e[12], e[13]); p1[3] = pk2(e[14], e[15]);
    ob = mma32(v1, p1, ob);
#pragma unroll
    for (int i = 0; i < 3; ++i) mine[64 * i] = (f32x4){oa[4 * i], oa[4 * i + 1], oa[4 * i + 2], oa[4 * i + 3]};
    WAITL_BAR();
    PRIO(3);
    f32x16 o;
#pragma unroll
    for (int i = 0; i < 3; ++i) {
        const f32x4 t = other[64 * i];
#pragma unroll
        for (int c = 0; c < 4; ++c) o[4 * i + c] = ob[4 * i + c] + t[c];
    }
    float l = upper(o[8]);
    const float mag = ((fabsf(o[0]) + fabsf(o[1])) + (fabsf(o[2]) + fabsf(o[3]))) + ((fabsf(o[4]) + fabsf(o[5])) + (fabsf(o[6]) + fabsf(o[7]))) + ((fabsf(o[8]) + fabsf(o[9])) + (fabsf(o[10]) + fabsf(o[11])));
    if (__builtin_expect(__any(!(mag * 0x1p-20f + l < 0x1p100f) || !(l > 0x1p-40f)), 0)) {
        if (hh) q2b[0] = 0u;
        float m = -INFINITY;
#pragma unroll 1
        for (int t = 0; t < 64; ++t) m = fmaxf(m, rowmax16(mma32(ld128(rs, l16, kv + t * REC), q1b, mma32k8(ld64(rs, l8, kv2 + t * REC), q2b, f32x16{}))));
        m = halfmax(m);
        o = f32x16{};
#pragma unroll 1
        for (int t = 0; t < 64; ++t) {
            const u32x4 va = ld128(rs, vg, kv + t * REC) | vfill, vb = ld128(rs, vg, kv + (t + 1) * REC + 768) | vfill;
            f32x16 s = mma32(ld128(rs, l16, kv + t * REC), q1b, mma32k8(ld64(rs, l8, kv2 + t * REC), q2b, f32x16{}));
#pragma unroll
            for (int i = 0; i < 16; ++i) s[i] = __builtin_amdgcn_exp2f(s[i] - m);
            o = mma32(vb, pk8(s, 1), mma32(va, pk8(s, 0), o));
        }
        l = upper(o[8]);
    }
    const float sc = __builtin_amdgcn_rcpf(l);
    st128(rs, l16, o1, pk8(o, 0, sc));
    st64(rs, hh ? WS_END : l16 >> 1, o2, (u32x2){pk2(o[8] * sc, o[9] * sc), pk2(o[10] * sc, o[11] * sc)});
}

DI void role_a(const char* x, const char* gw, char* ws, rsrc_t rs, int w, int lane, int b, int rb, int cg) {
    const int head = (b & 7) * 4 + ((b >> 3) & 3), qb = b >> 5;
    const u64 ep = ldflag(ws, WS_E);
    if (__builtin_expect(cg == 15, 0)) { gate_rows(x, gw, rs, rb); Flag(threadIdx.x == 0, FL + b, mktag(ep)).raise(rs); }
    if (w >= 4) {
        const unsigned i = threadIdx.x - 256, rec = i >> 4, c = (i & 3) == 0 ? 0x3f803f80u : 0u;
        *(LAS u32x4*)(uintptr_t)((rec >> 3) * 32768 + (rec & 7) * REC + 1536 + ((i >> 2) & 3) * 384 + 320 + (i & 3) * 16) = (u32x4){(i & 3) == 2 ? 0x3f80u : c, c, c, c};
        const Flag fq(threadIdx.x == 320, FQ + rb * 16 + cg, mktag(ep));
        WAITVL_BAR();
        fq.raise(rs);
    } else {
        if (cg < 10) qkv_compute<0>(rs, w, lane, rb, cg, b, ep); else if (cg < 15) qkv_compute<1>(rs, w, lane, rb, cg, b, ep);
        WAITV_BAR(0);
    }
    const u64 tag = mktag(ep);
    attention(ws, rs, w, lane, head, qb, tag);
    const Flag fo(threadIdx.x == 0, FO + qb * 32 + head, tag);
    WAITV_BAR(0);
    fo.raise(rs);
    if (b == 0) {
        await(ws, FQ + threadIdx.x, tag);
        BAR();
        if (threadIdx.x == 0) stflag(ws, WS_E, tag);
    }
}

DI void hid_compute(rsrc_t rs, int w, int lane, int rb, int cb16) {
    const unsigned rr = lane & 15, g = lane >> 4, rel = rr * 128 + ((g ^ (rr >> 1)) << 4);
    unsigned xa = w * 4096 + rel, wa = 49152 + rel;
    f32x4 acc[2][5];
#pragma unroll
    for (int i = 0; i < 10; ++i) { acc[i / 5][i % 5] = f32x4{}; PIN(acc[i / 5][i % 5]); }
    PIN(xa); PIN(wa);
    u32x4 f[2][7];
    auto rd = [&](int set, int ks, int i) { f[set][i] = i < 2 ? lds128((xa ^ (ks << 6)) + i * 2048) : lds128((wa ^ (ks << 6)) + (i - 2) * 2048); };
    WAITL_BAR();
    PRIO(3);
#pragma unroll
    for (int i = 0; i < 7; ++i) rd(0, 0, i);
    unsigned sb = 0;
#pragma unroll 2
    for (int s = 0; s < 10; ++s) {
#pragma unroll
        for (int ks = 0; ks < 2; ++ks) {
            if (ks) { WAITL_BAR(); const int d = sb == 2 ? -2 : 1; sb += d; xa += d * 16384; wa += d * 10240; }
            SBAR();
#pragma unroll
            for (int i = 0; i < 10; ++i) { acc[i & 1][i >> 1] = mma16(f[ks][i & 1], f[ks][2 + (i >> 1)], acc[i & 1][i >> 1]); if (i < 7) rd(ks ^ 1, ks ^ 1, i); SBAR(); }
        }
    }
    const unsigned so = WS_AB + ((cb16 >> 1) * 64 + rb * 4 + w) * 10240 + (cb16 & 1) * 5120;
#pragma unroll
    for (int j = 0; j < 5; ++j) st128(rs, lane * 16 + (j & 3) * 1024, so + (j >> 2) * 4096, pk44(acc[0][j], acc[1][j]));
}

DI void hid_load(const char* x, const char* wd, const char* ws, int li, int lane, int rb, int cb16, int b, u64 tag) {
    const int rot = ((rb & 7) * 5) >> 2;
    const unsigned row = lane >> 3, ev = swap23(row) * 1280 + (((lane & 7) ^ (row >> 1)) << 4);
    int left = 10 - rot;
    if (li < 2) {
        const unsigned v1 = (ev ^ 64) + 16 * 1280 - 1024;
        const char* base = x + (rb * 128 + 64 * li) * 1280 + rot * 128;
        unsigned lds = li * 8192;
        auto issue = [&]() {
#pragma unroll
            for (int i = 0; i < 4; ++i) dma2(ev, v1, base + ((i >> 1) * 32 + (i & 1) * 4) * 1280, lds + i * 2048);
            base += --left ? 128 : 128 - 1280; lds = lds >= 32768 ? lds - 32768 : lds + 16384;
        };
        await<NL>(ws, FL + b, tag);
#pragma unroll 1
        for (int s = 0; s < 3; ++s) { issue(); if (s == 1) WAITV_BAR(8); }
#pragma unroll 1
        for (int s = 0; s < 7; ++s) { WAITV_BAR(8); issue(); }
        WAITV_BAR(8); WAITV_BAR(0); BAR();
    } else {
        const unsigned o = li - 2;
        unsigned vw[5];
#pragma unroll
        for (int i = 0; i < 5; ++i) { const unsigned p = 5 * o + i; vw[i] = (p < 8 ? (ev ^ ((p & 1) << 6)) + (32 * (p >> 2) + 4 * ((p >> 1) & 1) + 16 * (p & 1)) * 1280 : ((ev & 127) ^ ((p & 1) << 6)) + (8 * p + row) * 1280) + 3072 - (i & 3) * 1024; }
        const char* base = wd + (cb16 & 7) * 80 * 1280 + rot * 128 - 3072;
        unsigned lds = 49152 + o * 5120;
        auto issue = [&]() { dma4(vw[0], vw[1], vw[2], vw[3], base, lds); dma1(vw[4], base, lds + 4096); base += --left ? 128 : 128 - 1280; lds = lds >= 49152 + 20480 ? lds - 20480 : lds + 10240; };
        __builtin_amdgcn_s_sleep(32);
#pragma unroll 1
        for (int s = 0; s < 3; ++s) issue();
        WAITV_BAR(10);
#pragma unroll 1
        for (int s = 0; s < 7; ++s) { WAITV_BAR(5); issue(); }
        WAITV_BAR(5); WAITV_BAR(0); BAR();
    }
}

DI void retile_wo(const char* wo, rsrc_t rs, int f, int lane) {
    const int cbk = f / 100, st = (f - cbk * 100) / 5, tile = f % 5, kh = st < 16 ? st >> 3 : (st >> 1) & 1, i = st < 16 ? st & 7 : 8 + (st & 1);
    const int rr = lane & 15, g = lane >> 4;
    const int e = cbk * 80 + (tile < 4 ? 32 * ((tile >> 1) ^ kh) + 8 * (rr >> 2) + 4 * (tile & 1) + (rr & 3) : 64 + rr);
    const int k0 = i < 8 ? (16 * kh + 2 * i + (g >> 1)) * 20 + 8 * (g & 1) : (16 * kh + 8 * (i - 8) + 2 * g) * 20 + 16, k4 = i < 8 ? k0 + 4 : k0 + 20;
    const unsigned short* p = (const unsigned short*)wo + e;
    u32x4 v;
    v[0] = p[k0 * 640] | ((unsigned)p[(k0 + 1) * 640] << 16); v[1] = p[(k0 + 2) * 640] | ((unsigned)p[(k0 + 3) * 640] << 16);
    v[2] = p[k4 * 640] | ((unsigned)p[(k4 + 1) * 640] << 16); v[3] = p[(k4 + 2) * 640] | ((unsigned)p[(k4 + 3) * 640] << 16);
    st128(rs, lane * 16, WS_WT + f * 1024, v);
}

DI void seq_compute(rsrc_t rs, int w, int lane, int kq, int mt, int nt, int hid, u64 tag) {
    const unsigned rr = lane & 15, g = lane >> 4;
    unsigned ac[2], ab = 49152 + (w >> 1) * 5120 + lane * 16;
    ac[0] = ((w & 1) * 64 + rr) * 128 + ((g ^ (rr >> 1)) << 4); ac[1] = ac[0] ^ 64;
    PIN(ac[0]); PIN(ac[1]); PIN(ab);
    f32x4 acc[4][5];
#pragma unroll
    for (int j = 0; j < 5; ++j) {
        f32x16 z = zero16(); PIN(z);
#pragma unroll
        for (int i = 0; i < 4; ++i) acc[i][j] = (f32x4){z[4 * i], z[4 * i + 1], z[4 * i + 2], z[4 * i + 3]};
    }
    u32x4 cf[4], af[3];
    WAITVL_BAR();
    if (w == 0) Flag(lane == 0, FW + hid, tag).raise(rs);
    PRIO(0);
#pragma unroll
    for (int i = 0; i < 4; ++i) cf[i] = lds128(ac[0] + i * 2048);
    af[0] = lds128(ab); af[1] = lds128(ab + 1024);
    auto step = [&](int k) {
        const int n = (k + 1) % 6;
#pragma unroll
        for (int j = 0; j < 5; ++j) {
            if (j == 3) WAITL_BAR();
            SBAR();
            af[(k * 5 + j + 2) % 3] = lds128(ab + (j < 3 ? (k % 3) * 10240 + (j + 2) * 1024 : (n % 3) * 10240 + (j - 3) * 1024));
            SBAR();
#pragma unroll
            for (int i0 = 0; i0 < 4; ++i0) {
                const int i = (j & 1) ? 3 - i0 : i0;
                acc[i][j] = mma16(af[(k * 5 + j) % 3], cf[i], acc[i][j]);
                if (j == 4) cf[i] = lds128(ac[n & 1] + (n >> 1) * 16384 + i * 2048);
                SBAR();
            }
        }
    };
#pragma unroll 1
    for (int t = 0; t < 5; ++t) {
        if (__builtin_amdgcn_readfirstlane(t) == GSW) PRIO(1);
#pragma unroll
        for (int k = 0; k < 6; ++k) step(k);
    }
    step(0); step(1);
    PRIO(3);
    const unsigned sb = WS_P + (kq * 256 + (mt * 2 + (w & 1)) * 8 + nt * 2 + (w >> 1)) * 10240;
    auto part = [&](int i) { return i < 8 ? pk44(acc[i >> 1][2 * (i & 1)], acc[i >> 1][2 * (i & 1) + 1]) : pk44(acc[2 * (i & 1)][4], acc[2 * (i & 1) + 1][4]); };
    if (w == kq) {
#pragma unroll
        for (int i = 0; i < 10; ++i) *(LAS u32x4*)(uintptr_t)(59392 + lane * 16 + i * 1024) = part(i);
    } else {
#pragma unroll
        for (int i = 0; i < 10; ++i) st128(rs, lane * 16 + (i & 3) * 1024, sb + (i >> 2) * 4096, part(i));
    }
}

DI void seq_load(const char* c, const char* ws, int li, int lane, int kq, int mt, int nt, int cb, unsigned fw, u64 tag, rsrc_t rs, u32x4 (&wl)[10]) {
    auto frags = [&]() {
#pragma unroll
        for (int i = 0; i < 10; ++i) wl[i] = ld128(rs, lane * 16 + (i & 3) * 1024, WS_WT + (cb * 100 + 90 + (i & ~3)) * 1024);
    };
    const char* wt = ws + WS_WT + cb * 102400;
    const int rot = (nt & 1) * 8;
    if (li < 2) {
        const unsigned row = lane >> 3, v0 = row * 4096 + (((lane & 7) ^ (row >> 1)) << 4), v1 = (v0 ^ 64) + 8 * 4096 - 1024;
        const char* base = c + (size_t)(mt * 128 + 64 * li) * 4096 + (kq & 1) * 2048 + rot * 128;
        unsigned lds = li * 8192; int left = 16 - rot;
        auto next = [&]() { lds = lds >= 32768 ? lds - 32768 : lds + 16384; };
        auto issue = [&]() {
#pragma unroll
            for (int i = 0; i < 4; ++i) dma2(v0, v1, base + i * 65536, lds + i * 2048);
            base += --left ? 128 : 128 - 2048; next();
        };
#pragma unroll 1
        for (int s = 0; s < 3; ++s) { issue(); if (s == 0) WAITV_BAR(0); }
#pragma unroll 1
        for (int s = 0; s < 13; ++s) { BAR(); WAITV_BAR(8); issue(); }
        frags();
#pragma unroll 1
        for (int s = 0; s < 3; ++s) { BAR(); WAITV_BAR(8); dma4c(lane * 16, wt + lds, lds); dma4c(lane * 16, wt + lds + 4096, lds + 4096); next(); }
    } else {
        const char* bb = ws + WS_AB + (((kq >> 1) * 4 + nt) * 128 + (kq & 1) * 64 + 4 * rot + li - 2) * 5120;
        unsigned lds = 49152 + (li - 2) * 5120; int left = 32 - 2 * rot;
        auto next = [&]() { lds = lds >= 49152 + 20480 ? lds - 20480 : lds + 10240; };
        auto issue = [&]() { dma5c(lane * 16, bb, lds); bb += --left ? 10240 : 10240 - 64 * 5120; next(); };
        await<NA>(ws, FA + ((kq & 1) * 8 + (lane >> 3)) * 16 + (kq >> 1) * 8 + 2 * nt + ((lane >> 2) & 1), tag);
#pragma unroll 1
        for (int s = 0; s < 2; ++s) issue();
        WAITV_BAR(5);
        issue();
#pragma unroll 1
        for (int s = 0; s < 20; ++s) { WAITV_BAR(5); issue(); }
        await(ws, fw, tag);
        frags();
        dma1c(lane * 16, wt + 79872 + (li - 2) * 1024, 79872 + (li - 2) * 1024);
#pragma unroll 1
        for (int s = 20; s < 29; ++s) { WAITV_BAR(5); issue(); }
#pragma unroll 1
        for (int s = 0; s < 2; ++s) { WAITV_BAR(5); dma5c(lane * 16, wt + lds + (s ? 0 : 81920 - 69632), lds); next(); }
        WAITV_BAR(5);
    }
}

DI void role_g(const char* x, const char* cr, const char* ci, const char* wr, const char* wi, const char* wo, char* ws, char* out, rsrc_t rs, int w, int lane, int b) {
    u64 tag = mktag(ldflag(ws, WS_E));
    PINS(tag);
    const int xcd = b & 7, idx = b >> 3;
    const int rb = (xcd & 1) * 8 + (idx & 7), cb16 = (xcd >> 1) * 4 + (idx >> 3), hid = rb * 16 + cb16;
    const int kq = xcd >> 1, mt = (xcd & 1) * 8 + (idx >> 2), nt = idx & 3;
    const int rbk = 2 * mt + (kq & 1), cb = 2 * nt + (kq >> 1);
    if (w < 4) hid_compute(rs, w, lane, rb, cb16);
    else hid_load(x, cb16 < 8 ? wr : wi, ws, w - 4, lane, rb, cb16, b, tag);
    PRIO(3);
    const Flag fa(threadIdx.x == 0, FA + hid, tag);
    WAITVL_BAR();
    fa.raise(rs);
    u32x4 wl[10];
    if (w < 4) {
        if (hid < 200) retile_wo(wo, rs, hid * 4 + w, lane);
        seq_compute(rs, w, lane, kq, mt, nt, hid, tag);
#pragma unroll
        for (int i = 0; i < 10; ++i) wl[i] = lds128(69632 + lane * 16 + i * 1024);
    } else seq_load(kq < 2 ? cr : ci, ws, w - 4, lane, kq, mt, nt, cb, lane < 25 ? FW + 25 * cb + lane : FQ + (rbk >> 1) * 16 + 15, tag, rs, wl);
    const unsigned ts = w & 3, kh = w >> 2, g = lane >> 4, tt = rbk * 2 + (ts >> 1), tq = (ts & 1) * 16 + (lane & 15), l16 = lane * 16;
    const float gate = __uint_as_float(ld32(rs, tq * 4, WS_G + tt * 128));
    const Flag fp(threadIdx.x == 0, FP + (mt * 4 + nt) * 4 + kq, tag);
    WAITVL_BAR();
    fp.raise(rs);
    if (w >= 6) { const unsigned a = 69632 + (w - 6) * 5120; dma5c(l16, ws + WS_WT + cb * 102400 + a, a); }
    u32x4 pa[4]; u32x2 pb[4];
    pa[0] = lds128(59392 + (2 * ts + kh) * 1024 + l16); pb[0] = lds64(59392 + (8 + (ts >> 1)) * 1024 + (ts & 1) * 8 + l16);
    if (w == 4) await<NA>(ws, FP + (mt * 4 + nt) * 4 + (lane >> 4), tag);
    WAITL_BAR();
    if (w >= 6) { const unsigned a = 59392 + (w - 6) * 5120; dma5c(l16, ws + WS_WT + cb * 102400 + a, a); }
    const unsigned ps = WS_P + (rbk * 8 + cb) * 10240;
#pragma unroll
    for (int q = 1; q < 4; ++q) { pa[q] = ld128(rs, l16, ps + (q ^ kq) * (256 * 10240) + (2 * ts + kh) * 1024); pb[q] = ld64(rs, l16, kh ? WS_END : ps + (q ^ kq) * (256 * 10240) + (8 + (ts >> 1)) * 1024 + (ts & 1) * 8); }
    SBAR();
    PRIO(0);
    SBAR();
    float y[12];
#pragma unroll
    for (int e = 0; e < 4; ++e) { y[2 * e] = (bflo(pa[0][e]) + bflo(pa[1][e])) - (bflo(pa[2][e]) + bflo(pa[3][e])); y[2 * e + 1] = (bfhi(pa[0][e]) + bfhi(pa[1][e])) - (bfhi(pa[2][e]) + bfhi(pa[3][e])); }
#pragma unroll
    for (int e = 0; e < 2; ++e) { y[8 + 2 * e] = (bflo(pb[0][e]) + bflo(pb[1][e])) - (bflo(pb[2][e]) + bflo(pb[3][e])); y[9 + 2 * e] = (bfhi(pb[0][e]) + bfhi(pb[1][e])) - (bfhi(pb[2][e]) + bfhi(pb[3][e])); }
#pragma unroll
    for (int i = 0; i < 12; ++i) { y[i] *= kq < 2 ? gate : -gate; PIN(y[i]); }
    float og = 1.f - gate;
    unsigned aw = kh * 40960 + l16, vo = g * 512 + tq * 16, vo2 = g * 512 + tq * 8;
    unsigned oa = (tt * 32 + (ts & 1) * 16 + (lane >> 2)) * 1280 + cb * 160 + kh * 64 + (lane & 3) * 16, ob = kh ? 2048 * 1280 : oa + 128 - (lane & 3) * 8, op = (lane & 3) * 64 + (lane & ~3);
    unsigned o1[4], o2 = WS_O2 + (tt * 32 + 16 * kh) * 256;
#pragma unroll
    for (int i = 0; i < 4; ++i) { o1[i] = WS_O1 + (tt * 32 + 16 * kh) * 1024 + i * 4096; PINS(o1[i]); }
    PIN(og); PIN(oa); PIN(ob); PIN(op); PIN(aw); PIN(vo); PIN(vo2); PINS(o2);
    if (w == 4) await<NO>(ws, FO + (rbk >> 2) * 32 + (lane >> 1), tag);
    WAITV_BAR(0);
    u32x4 of[10];
#pragma unroll
    for (int i = 0; i < 2; ++i) { const u32x2 lo = ld64(rs, vo2 + i * 2048, o2), hi = ld64(rs, vo2 + i * 2048 + 256, o2); of[i] = (u32x4){lo[0], lo[1], hi[0], hi[1]}; }
#pragma unroll
    for (int i = 0; i < 8; ++i) of[2 + i] = ld128(rs, vo + (i & 1) * 2048, o1[i >> 1]);
    SBAR();
    PRIO(3);
    f32x4 acc[5];
#pragma unroll
    for (int n = 0; n < 50; ++n) {
        acc[n % 5] = mma16(wl[n % 10], of[n / 5], n < 5 ? f32x4{} : acc[n % 5]);
        if (n < 40) wl[n % 10] = lds128(aw + n * 1024);
        if (n == 44) BAR();
        SBAR();
    }
    LAS f32x4* mine = (LAS f32x4*)(uintptr_t)(aw + ts * 3072);
    const LAS f32x4* other = (const LAS f32x4*)(uintptr_t)((aw ^ 40960) + ts * 3072);
    mine[0] = acc[2]; mine[64] = acc[3]; mine[128] = acc[4];
    WAITL_BAR();
    const f32x4 a0 = acc[0] + other[0], a1 = acc[1] + other[64], a2 = acc[4] + other[128];
    u32x4 r0 = {pk2(og * a0[0] + y[0], og * a0[1] + y[1]), pk2(og * a0[2] + y[2], og * a0[3] + y[3]), pk2(og * a1[0] + y[4], og * a1[1] + y[5]), pk2(og * a1[2] + y[6], og * a1[3] + y[7])};
    u32x2 r1 = {pk2(og * a2[0] + y[8], og * a2[1] + y[9]), pk2(og * a2[2] + y[10], og * a2[3] + y[11])};
#pragma unroll
    for (int i = 0; i < 4; ++i) r0[i] = __builtin_amdgcn_ds_bpermute(op, r0[i]);
#pragma unroll
    for (int i = 0; i < 2; ++i) r1[i] = __builtin_amdgcn_ds_bpermute(op, r1[i]);
    const rsrc_t ro = __builtin_amdgcn_make_buffer_rsrc(out, 0, 2048 * 1280, 0x00020000);
    __builtin_amdgcn_raw_buffer_store_b128(r0, ro, oa, 0, 0);
    __builtin_amdgcn_raw_buffer_store_b64(r1, ro, ob, 0, 0);
}

__global__ __launch_bounds__(512, 4) void fused_mix(const char* x, const char* cr, const char* ci, const char* wr, const char* wi, const char* wqkv, const char* wo, const char* gw, char* ws, char* out) {
    const int lane = threadIdx.x & 63, w = __builtin_amdgcn_readfirstlane(threadIdx.x >> 6);
    const rsrc_t rs = __builtin_amdgcn_make_buffer_rsrc(ws, 0, WS_END, 0x00020000);
    const int b = blockIdx.x, rb = (b & 1) * 8 + ((b >> 3) & 7), cg = ((b >> 1) & 3) + 4 * (b >> 6);
    if ((b >> 3) == 32 && w < 3) {
        const char* pc; asm volatile("s_getpc_b64 %0" : "=s"(pc));
        const unsigned t = *(const GAS unsigned*)(pc + min((unsigned)w * 8192 + lane * 128, CODE_BYTES));
        asm volatile("" :: "v"(t));
    }
    if (w >= 4 && cg < 15) { PRIO(3); qkv_load(x, wqkv, w - 4, lane, rb, cg); }
    if (b >= 256) { PRIO(2); role_g(x, cr, ci, wr, wi, wo, ws, out, rs, w, lane, b - 256); }
    else { PRIO(3); role_a(x, gw, ws, rs, w, lane, b, rb, cg); }
}

extern "C" void kernel_launch(void* const* d_in, const int* in_sizes, int n_in, void* d_out, int out_size, void* d_ws, size_t ws_size, hipStream_t stream) {
    (void)hipFuncSetAttribute((const void*)fused_mix, hipFuncAttributeMaxDynamicSharedMemorySize, LDS_BYTES);
    fused_mix<<<512, 512, LDS_BYTES, stream>>>((const char*)d_in[0], (const char*)d_in[1], (const char*)d_in[2], (const char*)d_in[3], (const char*)d_in[4],
                                               (const char*)d_in[6], (const char*)d_in[7], (const char*)d_in[8], (char*)d_ws, (char*)d_out);
}
```

```cpp
#include <hip/hip_runtime.h>
#include <hip/hip_bf16.h>
#include <stdint.h>

typedef __attribute__((ext_vector_type(8))) __bf16 bf16x8;
typedef __attribute__((ext_vector_type(2))) __bf16 bf16x2;
typedef __attribute__((ext_vector_type(4))) short s16x4;
typedef __attribute__((ext_vector_type(16))) float f32x16;
typedef __attribute__((ext_vector_type(4))) float f32x4;
typedef __attribute__((ext_vector_type(2))) float f32x2;
typedef __attribute__((ext_vector_type(4))) unsigned u32x4;
typedef __attribute__((ext_vector_type(2))) unsigned u32x2;
typedef unsigned long long u64;
typedef __amdgpu_buffer_rsrc_t rsrc_t;
#define LAS __attribute__((address_space(3)))
#define GAS __attribute__((address_space(1)))
#define DI __device__ __forceinline__
#define SBAR() __builtin_amdgcn_sched_barrier(0)
#define PIN(x) asm volatile("" : "+v"(x))
#define PINS(x) asm volatile("" : "+s"(x))
#define PRIO(n) __builtin_amdgcn_s_setprio(n)

constexpr unsigned REC = 3072, KV_HS = 65 * REC, QT = 1536, Q_HS = 64 * QT;
constexpr unsigned WS_KV = 0;
constexpr unsigned WS_Q = WS_KV + 32 * KV_HS;
constexpr unsigned WS_AB = WS_Q + 32 * Q_HS;
constexpr unsigned WS_P = WS_AB + 8 * 64 * 10240;
constexpr unsigned WS_O1 = WS_P + 4 * 256 * 10240;
constexpr unsigned WS_O2 = WS_O1 + 64 * 32 * 1024;
constexpr unsigned WS_WT = WS_O2 + 64 * 32 * 256;
constexpr unsigned WS_G = WS_WT + 800 * 1024;
constexpr unsigned WS_F = WS_G + 8192;
constexpr unsigned WS_E = WS_F + 6 * 256 * 128;
constexpr unsigned WS_END = WS_E + 128;
constexpr unsigned FQ = 0, FA = 256, FP = 512, FW = 768, FL = 1024, FO = 1280;
constexpr unsigned LDS_BYTES = 81920, CODE_BYTES = 24448;
constexpr float QSCALE = 0.22360679774997896f * 1.4426950408889634f;
#define PACE 3
#define AAT 6
#define NL 8
#define NA 8
#define NO 1
#define NTX 0
#define NTW 0
#define NTC 0
#define ROTA (5 * ((rb ^ (cg >> 2)) & 1))
#define WSL 32
#define GSW 2

DI unsigned swap23(unsigned r) { return (r & ~12u) | ((r & 4u) << 1) | ((r & 8u) >> 1); }
DI unsigned pk2(float a, float b) { f32x2 v = {a, b}; return __builtin_bit_cast(unsigned, __builtin_convertvector(v, bf16x2)); }
DI float bflo(unsigned u) { return __uint_as_float(u << 16); }
DI float bfhi(unsigned u) { return __uint_as_float(u & 0xffff0000u); }
DI f32x16 mma32(u32x4 a, u32x4 b, f32x16 c) { return __builtin_amdgcn_mfma_f32_32x32x16_bf16(__builtin_bit_cast(bf16x8, a), __builtin_bit_cast(bf16x8, b), c, 0, 0, 0); }
DI f32x16 mma32k8(u32x2 a, u32x2 b, f32x16 c) { return __builtin_amdgcn_mfma_f32_32x32x8bf16_1k(__builtin_bit_cast(s16x4, a), __builtin_bit_cast(s16x4, b), c, 0, 0, 0); }
DI f32x4 mma16(u32x4 a, u32x4 b, f32x4 c) { return __builtin_amdgcn_mfma_f32_16x16x32_bf16(__builtin_bit_cast(bf16x8, a), __builtin_bit_cast(bf16x8, b), c, 0, 0, 0); }
DI u32x4 pk8(const f32x16& a, int s, float c) { return (u32x4){pk2(a[8 * s] * c, a[8 * s + 1] * c), pk2(a[8 * s + 2] * c, a[8 * s + 3] * c), pk2(a[8 * s + 4] * c, a[8 * s + 5] * c), pk2(a[8 * s + 6] * c, a[8 * s + 7] * c)}; }
DI u32x4 pk8(const f32x16& a, int s) { return (u32x4){pk2(a[8 * s], a[8 * s + 1]), pk2(a[8 * s + 2], a[8 * s + 3]), pk2(a[8 * s + 4], a[8 * s + 5]), pk2(a[8 * s + 6], a[8 * s + 7])}; }
DI u32x4 pk44(f32x4 a, f32x4 b) { return (u32x4){pk2(a[0], a[1]), pk2(a[2], a[3]), pk2(b[0], b[1]), pk2(b[2], b[3])}; }
DI f32x16 zero16() { u32x2 z = {0u, 0u}; PIN(z); return mma32k8(z, z, f32x16{}); }
DI u32x4 lds128(unsigned a) { return *(const LAS u32x4*)(uintptr_t)a; }
DI u32x2 lds64(unsigned a) { return *(const LAS u32x2*)(uintptr_t)a; }
DI u32x2 ldstr(unsigned a) { return __builtin_bit_cast(u32x2, __builtin_amdgcn_ds_read_tr16_b64_v4i16((LAS s16x4*)(uintptr_t)a)); }
DI void st128(rsrc_t rs, unsigned v, unsigned s, u32x4 d) { __builtin_amdgcn_raw_buffer_store_b128(d, rs, v, s, 16); asm volatile("s_nop 1" :: "v"(d) : "memory"); }
DI void st64(rsrc_t rs, unsigned v, unsigned s, u32x2 d) { __builtin_amdgcn_raw_buffer_store_b64(d, rs, v, s, 16); }
DI void st32(rsrc_t rs, unsigned v, unsigned s, unsigned d) { __builtin_amdgcn_raw_buffer_store_b32(d, rs, v, s, 16); }
DI u32x4 ld128(rsrc_t rs, unsigned v, unsigned s) { return __builtin_amdgcn_raw_buffer_load_b128(rs, v, s, 16); }
DI u32x2 ld64(rsrc_t rs, unsigned v, unsigned s) { return __builtin_amdgcn_raw_buffer_load_b64(rs, v, s, 16); }
DI unsigned ld32(rsrc_t rs, unsigned v, unsigned s) { return __builtin_amdgcn_raw_buffer_load_b32(rs, v, s, 16); }
#define M0SET "s_mov_b32 m0, %[l]\n\ts_nop 0\n\t"
#define GLD(v, o, c) "global_load_lds_dwordx4 %[" v "], %[b] offset:" o c "\n\t"
DI void dma1(unsigned v0, const char* base, unsigned lds) { asm volatile(M0SET GLD("v0", "0", "") ::[v0] "v"(v0), [b] "s"(base), [l] "s"(lds) : "memory"); }
DI void dma2(unsigned v0, unsigned v1, const char* base, unsigned lds) { asm volatile(M0SET GLD("v0", "0", "") GLD("v1", "1024", "") ::[v0] "v"(v0), [v1] "v"(v1), [b] "s"(base), [l] "s"(lds) : "memory"); }
DI void dma4(unsigned v0, unsigned v1, unsigned v2, unsigned v3, const char* base, unsigned lds) {
    asm volatile(M0SET GLD("v0", "0", "") GLD("v1", "1024", "") GLD("v2", "2048", "") GLD("v3", "3072", "") ::[v0] "v"(v0), [v1] "v"(v1), [v2] "v"(v2), [v3] "v"(v3), [b] "s"(base), [l] "s"(lds) : "memory");
}
DI void dma2n(unsigned v0, unsigned v1, const char* base, unsigned lds) { asm volatile(M0SET GLD("v0", "0", " nt") GLD("v1", "1024", " nt") ::[v0] "v"(v0), [v1] "v"(v1), [b] "s"(base), [l] "s"(lds) : "memory"); }
DI void dma4n(unsigned v0, unsigned v1, unsigned v2, unsigned v3, const char* base, unsigned lds) {
    asm volatile(M0SET GLD("v0", "0", " nt") GLD("v1", "1024", " nt") GLD("v2", "2048", " nt") GLD("v3", "3072", " nt") ::[v0] "v"(v0), [v1] "v"(v1), [v2] "v"(v2), [v3] "v"(v3), [b] "s"(base), [l] "s"(lds) : "memory");
}
DI void dma1c(unsigned v0, const char* base, unsigned lds) { asm volatile(M0SET GLD("v0", "0", " sc1") ::[v0] "v"(v0), [b] "s"(base), [l] "s"(lds) : "memory"); }
DI void dma4c(unsigned v0, const char* base, unsigned lds) {
    asm volatile(M0SET GLD("v0", "0", " sc1") GLD("v0", "1024", " sc1") GLD("v0", "2048", " sc1") GLD("v0", "3072", " sc1") ::[v0] "v"(v0), [b] "s"(base), [l] "s"(lds) : "memory");
}
DI void dma5c(unsigned v0, const char* base, unsigned lds) { dma4c(v0, base, lds); dma1c(v0, base + 4096, lds + 4096); }
DI void dma_rec(unsigned v0, unsigned v1, const char* base, unsigned lds) {
    asm volatile(M0SET GLD("v0", "0", " sc1") "s_mov_b32 exec_hi, 0xff0fffff\n\t" GLD("v1", "1024", " sc1") "s_mov_b32 exec_lo, 0xffff0fff\n\ts_mov_b32 exec_hi, 0x0fffff0f\n\t" GLD("v0", "2048", " sc1") "s_mov_b64 exec, -1"
                 ::[v0] "v"(v0), [v1] "v"(v1), [b] "s"(base), [l] "s"(lds) : "memory");
}
#define BAR() asm volatile("s_barrier" ::: "memory")
#define WAITV_BAR(N) asm volatile("s_waitcnt vmcnt(" #N ")\n\ts_barrier" ::: "memory")
#define WAITL_BAR() asm volatile("s_waitcnt lgkmcnt(0)\n\ts_barrier" ::: "memory")
#define WAITVL_BAR() asm volatile("s_waitcnt vmcnt(0) lgkmcnt(0)\n\ts_barrier" ::: "memory")

DI u64 ldflag(const char* ws, unsigned off) { return __hip_atomic_load((const GAS u64*)(ws + off), __ATOMIC_RELAXED, __HIP_MEMORY_SCOPE_AGENT); }
DI void stflag(char* ws, unsigned off, u64 v) { __hip_atomic_store((GAS u64*)(ws + off), v, __ATOMIC_RELAXED, __HIP_MEMORY_SCOPE_AGENT); }
DI u64 mktag(u64 e) {
    const u64 u = ((u64)(unsigned)__builtin_amdgcn_readfirstlane((unsigned)(e >> 32)) << 32) | (unsigned)__builtin_amdgcn_readfirstlane((unsigned)e);
    return u * 6364136223846793005ull + 1442695040888963407ull;
}
struct Flag {
    unsigned v; u32x2 d;
    DI Flag(bool mine, unsigned f, u64 tag) { v = mine ? WS_F + f * 128 : WS_END; d = (u32x2){(unsigned)tag, (unsigned)(tag >> 32)}; PIN(v); PIN(d); }
    DI void raise(rsrc_t rs) const { st64(rs, v, 0, d); }
};
template <int NAP = 1> DI void await(const char* ws, unsigned f0, u64 tag) {
    u64 a; unsigned n = 1u << 19;
    asm volatile("1:\n\tglobal_load_dwordx2 %0, %2, %3 sc1\n\ts_sub_u32 %1, %1, 1\n\ts_sleep %c5\n\ts_waitcnt vmcnt(0)\n\tv_cmp_ne_u64 vcc, %4, %0\n\ts_cbranch_scc1 2f\n\ts_cbranch_vccnz 1b\n2:"
                 : "=&v"(a), "+s"(n) : "v"(WS_F + f0 * 128), "s"(ws), "s"(tag), "i"(NAP) : "vcc", "scc", "memory");
}
DI void await(const char* ws, unsigned f0, unsigned f1, u64 tag) {
    u64 a, b, m; unsigned n = 1u << 19;
    asm volatile("1:\n\tglobal_load_dwordx2 %0, %4, %6 sc1\n\tglobal_load_dwordx2 %1, %5, %6 sc1\n\ts_sub_u32 %3, %3, 1\n\ts_cbranch_scc1 2f\n\ts_waitcnt vmcnt(0)\n\t"
                 "v_cmp_ne_u64 %2, %7, %0\n\tv_cmp_ne_u64 vcc, %7, %1\n\ts_or_b64 vcc, vcc, %2\n\ts_cbranch_scc1 1b\n2:\n\ts_waitcnt vmcnt(0)"
                 : "=&v"(a), "=&v"(b), "=&s"(m), "+s"(n) : "v"(WS_F + f0 * 128), "v"(WS_F + f1 * 128), "s"(ws), "s"(tag) : "vcc", "scc", "memory");
}

template <int T>
DI void qkv_compute(rsrc_t rs, int w, int lane, int rb, int cg, int b, u64 ep) {
    const unsigned r = lane & 31, hh = lane >> 5, wm = w & 1, wn = w >> 1, q = (lane & 15) >> 2, p = lane & 3;
    const unsigned pp = T == 0 ? ((p & 1) << 1) | (p >> 1) : p;
    unsigned xa = (64 * wm + r) * 128 + ((hh ^ ((r >> 1) & 7)) << 4), wa[2];
    wa[0] = 49152 + (8 * hh + q) * 256 + (((2 * wn) ^ q) << 6) + ((lane >> 4) & 1) * 32 + pp * 8; wa[1] = wa[0] ^ 64;
    PIN(xa); PIN(wa[0]); PIN(wa[1]);
    const unsigned kt = rb * 4 + 2 * wm;
    const bool isk = cg >= 5;
    unsigned va[4], sb, ts = REC;
    if (T == 0) {
        const unsigned ct = isk ? cg - 5 : cg, hs = isk ? KV_HS : Q_HS, slot = isk ? r : swap23(r);
        ts = isk ? REC : QT; sb = (isk ? WS_KV : WS_Q) + kt * ts;
#pragma unroll
        for (int i = 0; i < 4; ++i) { const unsigned oc = ct * 16 + 8 * wn + 2 * i + hh, hp = oc / 5, ty = oc - hp * 5; va[i] = (2 * hp + (ty > 2)) * hs + (ty > 2 ? ty - 3 : ty) * 512 + slot * 16; PIN(va[i]); }
    } else {
        sb = WS_KV + kt * REC + 1536;
#pragma unroll
        for (int i = 0; i < 2; ++i) { const unsigned c = (cg - 10) * 128 + 64 * wn + 32 * i + r, head = c / 20, d = c - head * 20; va[i] = head * KV_HS + hh * 384 + (d < 16 ? swap23(d) : d) * 16; PIN(va[i]); }
    }
    f32x16 acc[2][2];
#pragma unroll
    for (int i = 0; i < 4; ++i) { acc[i >> 1][i & 1] = zero16(); PIN(acc[i >> 1][i & 1]); }
    u32x4 xf[2][2], wf[2][2];
    auto rd_x = [&](int set, int g, int mi) { xf[set][mi] = lds128((xa ^ ((g & 3) << 5)) + mi * 4096); };
    auto rd_w = [&](int set, int g, int ni) {
        const unsigned a = wa[ni] + (g & 7) * 4096;
        const u32x2 lo = ldstr(a), hi = ldstr(a + 1024); wf[set][ni] = (u32x4){lo[0], lo[1], hi[0], hi[1]};
    };
    WAITL_BAR();
    rd_x(0, 0, 0); rd_w(0, 0, 0); rd_x(0, 0, 1); rd_w(0, 0, 1);
    unsigned xs = 0;
#pragma unroll 1
    for (int t = 0; t < 5; ++t) {
        if (__builtin_expect(t == PACE && w == 0, 0)) Flag(lane == 0, FL + b, mktag(ep)).raise(rs);
#pragma unroll
        for (int g = 0; g < 8; ++g) {
            const int c = g & 1, n = c ^ 1;
            if (c) WAITL_BAR();
            if ((g & 3) == 3) { const unsigned d = xs == 32768 ? -32768 : 16384; xs += d; xa += d; }
            SBAR();
#pragma unroll
            for (int i = 0; i < 4; ++i) {
                const int mi = i & 1, ni = i >> 1;
                acc[mi][ni] = T == 0 ? mma32(wf[c][ni], xf[c][mi], acc[mi][ni]) : mma32(xf[c][mi], wf[c][ni], acc[mi][ni]);
                if (i == 0) { rd_x(n, g + 1, 0); rd_w(n, g + 1, 0); }
                if (i == 1) { rd_x(n, g + 1, 1); rd_w(n, g + 1, 1); }
                SBAR();
            }
        }
    }
    const float sc = isk ? 1.f : QSCALE;
#pragma unroll
    for (int i = 0; i < 8; ++i) {
        const int mi = (i >> 1) & 1, ni = i >> 2, s = i & 1;
        if (T == 0) st128(rs, va[2 * ni + s], sb + mi * ts, pk8(acc[mi][ni], s, sc));
        else st128(rs, va[ni], sb + (mi + s) * REC + s * 768, pk8(acc[mi][ni], s));
    }
}

DI void qkv_load(const char* x, const char* wqkv, int li, int lane, int rb, int cg) {
    const int rot = ROTA;
    if (li >= 2) {
        unsigned v0 = (lane >> 4) * 3840 + ((((lane >> 2) & 3) ^ (lane >> 4)) << 6) + (lane & 3) * 16, v1 = v0 + 14336, v2 = v0 + 2 * 14336, v3 = v0 + 3 * 14336;
        unsigned off = cg * 256 + (16 * (li - 2) + 64 * rot) * 3840, lds = 49152 + (li - 2) * 4096;
        PIN(v0); PIN(v1); PIN(v2); PIN(v3); PINS(off); PINS(lds); SBAR();
        const char* base = wqkv + off;
        unsigned slot = 0; int left = 20 - 2 * rot;
        auto issue = [&]() { if (NTW) dma4n(v0, v1, v2, v3, base, lds + slot); else dma4(v0, v1, v2, v3, base, lds + slot); base += --left ? 32 * 3840 : (32 - 640) * 3840; slot = (slot + 8192) & 32767; };
        issue(); issue();
        WAITV_BAR(4);
        issue();
#pragma unroll 1
        for (int s = 0; s < 17; ++s) { issue(); WAITV_BAR(8); }
        WAITV_BAR(4); WAITV_BAR(0); BAR();
    } else {
        const unsigned row = lane >> 3;
        unsigned v0 = swap23(row) * 1280 + (((lane & 7) ^ (row >> 1)) << 4), v1 = (v0 ^ 64) + 4 * 1280 - 1024;
        unsigned off = (rb * 128 + 64 * li) * 1280 + rot * 128, lds = li * 8192;
        PIN(v0); PIN(v1); PINS(off); PINS(lds); SBAR();
        const char* base = x + off;
        int left = 10 - rot;
        auto issue = [&]() {
#pragma unroll
            for (int i = 0; i < 4; ++i) { if (NTX) dma2n(v0, v1, base + i * 16 * 1280, lds + i * 2048); else dma2(v0, v1, base + i * 16 * 1280, lds + i * 2048); }
            base += --left ? 128 : 128 - 1280; lds = lds >= 32768 ? lds - 32768 : lds + 16384;
        };
#pragma unroll 1
        for (int s = 0; s < 2; ++s) issue();
        WAITV_BAR(8);
#pragma unroll 1
        for (int s = 0; s < 8; ++s) { issue(); BAR(); WAITV_BAR(8); }
        BAR(); WAITV_BAR(0); BAR(); BAR();
    }
}

DI void gate_rows(const char* x, const char* gw, rsrc_t rs, int rb) {
    const int row = rb * 128 + (threadIdx.x >> 2), sub = threadIdx.x & 3;
    float s = 0.f;
#pragma unroll 5
    for (int i = 0; i < 20; ++i) {
        const u32x4 a = *(const u32x4*)(x + row * 1280 + (4 * i + sub) * 16), g = *(const u32x4*)(gw + (4 * i + sub) * 16);
#pragma unroll
        for (int e = 0; e < 4; ++e) s += bflo(a[e]) * bflo(g[e]) + bfhi(a[e]) * bfhi(g[e]);
    }
    s += __shfl_xor(s, 1); s += __shfl_xor(s, 2);
    if (sub == 0) st32(rs, row * 4, WS_G, __float_as_uint(1.f / (1.f + __expf(-s))));
}

DI float rowmax16(const f32x16& s) {
    float m = fmaxf(fmaxf(s[0], s[1]), s[2]);
#pragma unroll
    for (int i = 3; i < 15; i += 2) m = fmaxf(fmaxf(m, s[i]), s[i + 1]);
    return fmaxf(m, s[15]);
}
DI float halfmax(float m) { const auto t = __builtin_amdgcn_permlane32_swap(__float_as_uint(m), __float_as_uint(m), false, false); return fmaxf(__uint_as_float(t[0]), __uint_as_float(t[1])); }
DI float upper(float m) { const auto t = __builtin_amdgcn_permlane32_swap(__float_as_uint(m), __float_as_uint(m), false, false); return __uint_as_float(t[1]); }

DI void attention(char* ws, rsrc_t rs, int w, int lane, int head, int qb, u64 tag) {
    const unsigned r = lane & 31, hh = lane >> 5, kh = w >> 2, qt = qb * 8 + 2 * (w & 3), odd = head & 1;
    const unsigned kv = WS_KV + head * KV_HS, kv2 = kv - odd * KV_HS + 1024;
    unsigned l16 = lane * 16, l8 = hh ? WS_END : r * 16 + odd * 8, d0 = l16 + KV_HS, d1 = hh ? d0 : d0 - odd * KV_HS;
    unsigned vg = r < 20 ? 1536 + hh * 384 + r * 16 : WS_END, vfill = r == 20 ? 0x3f803f80u : 0u, kone = hh ? 0x3f80u : 0u;
    const char* src = ws + kv + (32 * kh + 1 + (w & 3)) * REC - KV_HS;
    unsigned dst = w * REC, kvh = kv + 32 * kh * REC, kv2h = kv2 + 32 * kh * REC;
    unsigned qa = WS_Q + head * Q_HS + (qt + 1 - kh) * QT, qa2 = qa + 1024 - odd * Q_HS;
    unsigned o1 = WS_O1 + ((qt + kh) * 32 + head) * 1024, o2 = WS_O2 + ((qt + kh) * 32 + head) * 256;
    const unsigned lb = kh * 4 * REC;
    unsigned ak1 = lb + l16, ak2 = lb + (hh ? 1536 + 22 * 16 : 1024 + r * 16 + odd * 8), av = lb + 1536 + (hh ^ (r > 27)) * 384 + min(r, 21u) * 16;
    unsigned xm = 57344 + w * 3072 + l16, xo = 57344 + (w ^ 4) * 3072 + l16;
    u32x4 p0, p1 = u32x4{}, v1 = u32x4{};
    f32x16 oa = zero16(), ob = zero16();
    float e[16]; e[12] = 0.f; e[13] = 0.f; e[14] = 0.f; e[15] = 0.f;
    PIN(l16); PIN(l8); PIN(d0); PIN(d1); PIN(vg); PIN(vfill); PIN(kone); PIN(ak1); PIN(ak2); PIN(av); PIN(xm); PIN(xo);
    PIN(p1); PIN(v1); PIN(oa); PIN(ob); PIN(e[12]); PIN(e[13]); PIN(e[14]); PIN(e[15]);
    PINS(src); PINS(dst); PINS(kvh); PINS(kv2h); PINS(qa); PINS(qa2); PINS(o1); PINS(o2);
    if (w == 4) {
        const unsigned c0 = (20 * head) >> 7, c1 = (20 * head + 19) >> 7;
        await(ws, FQ + ((lane >> 1) & 15) * 16 + 5 + 5 * (lane >> 5) + ((lane & 1) ? c1 : c0), FQ + (2 * qb + (lane >> 5)) * 16 + ((lane & 16) ? c1 : c0), tag);
    }
    BAR();
    u32x2 k2 = ld64(rs, l8, kv2h), q2a = ld64(rs, l8, qa2);
    u32x4 k1 = ld128(rs, l16, kvh);
    const u32x4 q1a = ld128(rs, l16, qa);
    SBAR();
    u32x4 v0 = ld128(rs, vg, kvh);
    dma_rec(d0, d1, src, dst);
    SBAR();
    f32x16 sa = mma32(k1, q1a, mma32k8(k2, q2a, f32x16{})), sb;
    *(LAS u32x4*)(uintptr_t)(xm + 1024) = q1a;
    unsigned na = pk2(-halfmax(rowmax16(sa)), 0.f); PIN(na);
    q2a[0] = hh ? na : q2a[0]; k2[0] |= kone;
    *(LAS u32x2*)(uintptr_t)xm = q2a;
    sa = mma32(k1, q1a, mma32k8(k2, q2a, f32x16{}));
    SBAR();
    WAITL_BAR();
    u32x2 q2b = lds64(xo);
    const u32x4 q1b = lds128(xo + 1024);
    v0 |= vfill;
    const int jl = kh ? 7 : 0;
#define EX(C, i) do { e[i] = __builtin_amdgcn_exp2f(C[i]); PIN(e[i]); SBAR(); } while (0)
#define CV(P, n, i) do { unsigned t_ = pk2(e[i], e[i + 1]); PIN(t_); P[n] = t_; SBAR(); } while (0)
#define UNIT(C, N, O, OP, Q1, Q2, u)                                                                          \
    do {                                                                                                      \
        SBAR();                                                                                               \
        N = mma32k8(k2, Q2, f32x16{}); SBAR();                                                                \
        if (u == AAT) PRIO(0);                                                                                \
        if (u == 0) {                                                                                         \
            WAITVL_BAR(); PRIO(2);                                                                            \
            if (__builtin_expect(j + jj < jl, 0)) { src += 4 * REC; dma_rec(d0, d1, src - 32 * REC, dst - 4 * REC + 32768 - fl); dma_rec(d0, d1, src, dst + 32768 - fl); } \
        }                                                                                                     \
        if (!(u & 1)) { k2 = lds64(ak2 + (u >> 1) * REC + fl); SBAR(); }                                      \
        EX(C, 0); CV(p1, 2, 12); EX(C, 1); CV(p1, 3, 14); EX(C, 2); EX(C, 3);                                 \
        OP = mma32(v1, p1, OP); SBAR();                                                                       \
        if (!(u & 1)) { v1 = lds128(av + 768 + (u >> 1) * REC + fl); SBAR(); }                                \
        EX(C, 4); CV(p0, 0, 0); EX(C, 5); CV(p0, 1, 2); EX(C, 6); EX(C, 7);                                   \
        N = mma32(k1, Q1, N); SBAR();                                                                         \
        if (!(u & 1)) { k1 = lds128(ak1 + (u >> 1) * REC + fl); SBAR(); }                                     \
        EX(C, 8); CV(p0, 2, 4); EX(C, 9); CV(p0, 3, 6); EX(C, 10); EX(C, 11);                                 \
        O = mma32(v0, p0, O); SBAR();                                                                         \
        if (u & 1) { v0 = lds128(av + (u >> 1) * REC + fl); SBAR(); }                                         \
        EX(C, 12); CV(p1, 0, 8); EX(C, 13); CV(p1, 1, 10); EX(C, 14); EX(C, 15);                              \
    } while (0)
#pragma unroll 1
    for (int j = 0; j < 8; j += 2) {
#pragma unroll
        for (int jj = 0; jj < 2; ++jj) {
            const unsigned fl = jj * 32768;
            UNIT(sa, sb, oa, ob, q1b, q2b, 0); UNIT(sb, sa, ob, oa, q1a, q2a, 1); UNIT(sa, sb, oa, ob, q1b, q2b, 2); UNIT(sb, sa, ob, oa, q1a, q2a, 3);
            UNIT(sa, sb, oa, ob, q1b, q2b, 4); UNIT(sb, sa, ob, oa, q1a, q2a, 5); UNIT(sa, sb, oa, ob, q1b, q2b, 6); UNIT(sb, sa, ob, oa, q1a, q2a, 7);
        }
    }
#undef UNIT
#undef EX
#undef CV
    LAS f32x4* mine = (LAS f32x4*)(uintptr_t)xm;
    const LAS f32x4* other = (const LAS f32x4*)(uintptr_t)xo;
    p1[2] = pk2(e[12], e[13]); p1[3] = pk2(e[14], e[15]);
    ob = mma32(v1, p1, ob);
#pragma unroll
    for (int i = 0; i < 3; ++i) mine[64 * i] = (f32x4){oa[4 * i], oa[4 * i + 1], oa[4 * i + 2], oa[4 * i + 3]};
    WAITL_BAR();
    PRIO(3);
    f32x16 o;
#pragma unroll
    for (int i = 0; i < 3; ++i) {
        const f32x4 t = other[64 * i];
#pragma unroll
        for (int c = 0; c < 4; ++c) o[4 * i + c] = ob[4 * i + c] + t[c];
    }
    float l = upper(o[8]);
    const float mag = ((fabsf(o[0]) + fabsf(o[1])) + (fabsf(o[2]) + fabsf(o[3]))) + ((fabsf(o[4]) + fabsf(o[5])) + (fabsf(o[6]) + fabsf(o[7]))) + ((fabsf(o[8]) + fabsf(o[9])) + (fabsf(o[10]) + fabsf(o[11])));
    if (__builtin_expect(__any(!(mag * 0x1p-20f + l < 0x1p100f) || !(l > 0x1p-40f)), 0)) {
        if (hh) q2b[0] = 0u;
        float m = -INFINITY;
#pragma unroll 1
        for (int t = 0; t < 64; ++t) m = fmaxf(m, rowmax16(mma32(ld128(rs, l16, kv + t * REC), q1b, mma32k8(ld64(rs, l8, kv2 + t * REC), q2b, f32x16{}))));
        m = halfmax(m);
        o = f32x16{};
#pragma unroll 1
        for (int t = 0; t < 64; ++t) {
            const u32x4 va = ld128(rs, vg, kv + t * REC) | vfill, vb = ld128(rs, vg, kv + (t + 1) * REC + 768) | vfill;
            f32x16 s = mma32(ld128(rs, l16, kv + t * REC), q1b, mma32k8(ld64(rs, l8, kv2 + t * REC), q2b, f32x16{}));
#pragma unroll
            for (int i = 0; i < 16; ++i) s[i] = __builtin_amdgcn_exp2f(s[i] - m);
            o = mma32(vb, pk8(s, 1), mma32(va, pk8(s, 0), o));
        }
        l = upper(o[8]);
    }
    const float sc = __builtin_amdgcn_rcpf(l);
    st128(rs, l16, o1, pk8(o, 0, sc));
    st64(rs, hh ? WS_END : l16 >> 1, o2, (u32x2){pk2(o[8] * sc, o[9] * sc), pk2(o[10] * sc, o[11] * sc)});
}

DI void role_a(const char* x, const char* gw, char* ws, rsrc_t rs, int w, int lane, int b, int rb, int cg) {
    const int head = (b & 7) * 4 + ((b >> 3) & 3), qb = b >> 5;
    const u64 ep = ldflag(ws, WS_E);
    if (__builtin_expect(cg == 15, 0)) { gate_rows(x, gw, rs, rb); Flag(threadIdx.x == 0, FL + b, mktag(ep)).raise(rs); }
    if (w >= 4) {
        const unsigned i = threadIdx.x - 256, rec = i >> 4, c = (i & 3) == 0 ? 0x3f803f80u : 0u;
        *(LAS u32x4*)(uintptr_t)((rec >> 3) * 32768 + (rec & 7) * REC + 1536 + ((i >> 2) & 3) * 384 + 320 + (i & 3) * 16) = (u32x4){(i & 3) == 2 ? 0x3f80u : c, c, c, c};
        const Flag fq(threadIdx.x == 320, FQ + rb * 16 + cg, mktag(ep));
        WAITVL_BAR();
        fq.raise(rs);
    } else {
        if (cg < 10) qkv_compute<0>(rs, w, lane, rb, cg, b, ep); else if (cg < 15) qkv_compute<1>(rs, w, lane, rb, cg, b, ep);
        WAITV_BAR(0);
    }
    const u64 tag = mktag(ep);
    attention(ws, rs, w, lane, head, qb, tag);
    const Flag fo(threadIdx.x == 0, FO + qb * 32 + head, tag);
    WAITV_BAR(0);
    fo.raise(rs);
    if (b == 0) {
        await(ws, FQ + threadIdx.x, tag);
        BAR();
        if (threadIdx.x == 0) stflag(ws, WS_E, tag);
    }
}

DI void hid_compute(rsrc_t rs, int w, int lane, int rb, int cb16) {
    const unsigned rr = lane & 15, g = lane >> 4, rel = rr * 128 + ((g ^ (rr >> 1)) << 4);
    unsigned xa = w * 4096 + rel, wa = 49152 + rel;
    f32x4 acc[2][5];
#pragma unroll
    for (int i = 0; i < 10; ++i) { acc[i / 5][i % 5] = f32x4{}; PIN(acc[i / 5][i % 5]); }
    PIN(xa); PIN(wa);
    u32x4 f[2][7];
    auto rd = [&](int set, int ks, int i) { f[set][i] = i < 2 ? lds128((xa ^ (ks << 6)) + i * 2048) : lds128((wa ^ (ks << 6)) + (i - 2) * 2048); };
    WAITL_BAR();
    PRIO(3);
#pragma unroll
    for (int i = 0; i < 7; ++i) rd(0, 0, i);
    unsigned sb = 0;
#pragma unroll 2
    for (int s = 0; s < 10; ++s) {
#pragma unroll
        for (int ks = 0; ks < 2; ++ks) {
            if (ks) { WAITL_BAR(); const int d = sb == 2 ? -2 : 1; sb += d; xa += d * 16384; wa += d * 10240; }
            SBAR();
#pragma unroll
            for (int i = 0; i < 10; ++i) { acc[i & 1][i >> 1] = mma16(f[ks][i & 1], f[ks][2 + (i >> 1)], acc[i & 1][i >> 1]); if (i < 7) rd(ks ^ 1, ks ^ 1, i); SBAR(); }
        }
    }
    const unsigned so = WS_AB + ((cb16 >> 1) * 64 + rb * 4 + w) * 10240 + (cb16 & 1) * 5120;
#pragma unroll
    for (int j = 0; j < 5; ++j) st128(rs, lane * 16 + (j & 3) * 1024, so + (j >> 2) * 4096, pk44(acc[0][j], acc[1][j]));
}

DI void hid_load(const char* x, const char* wd, const char* ws, int li, int lane, int rb, int cb16, int b, u64 tag) {
    const int rot = ((rb & 7) * 5) >> 2;
    const unsigned row = lane >> 3, ev = swap23(row) * 1280 + (((lane & 7) ^ (row >> 1)) << 4);
    int left = 10 - rot;
    if (li < 2) {
        const unsigned v1 = (ev ^ 64) + 16 * 1280 - 1024;
        const char* base = x + (rb * 128 + 64 * li) * 1280 + rot * 128;
        unsigned lds = li * 8192;
        auto issue = [&]() {
#pragma unroll
            for (int i = 0; i < 4; ++i) dma2(ev, v1, base + ((i >> 1) * 32 + (i & 1) * 4) * 1280, lds + i * 2048);
            base += --left ? 128 : 128 - 1280; lds = lds >= 32768 ? lds - 32768 : lds + 16384;
        };
        await<NL>(ws, FL + b, tag);
#pragma unroll 1
        for (int s = 0; s < 3; ++s) { issue(); if (s == 1) WAITV_BAR(8); }
#pragma unroll 1
        for (int s = 0; s < 7; ++s) { WAITV_BAR(8); issue(); }
        WAITV_BAR(8); WAITV_BAR(0); BAR();
    } else {
        const unsigned o = li - 2;
        unsigned vw[5];
#pragma unroll
        for (int i = 0; i < 5; ++i) { const unsigned p = 5 * o + i; vw[i] = (p < 8 ? (ev ^ ((p & 1) << 6)) + (32 * (p >> 2) + 4 * ((p >> 1) & 1) + 16 * (p & 1)) * 1280 : ((ev & 127) ^ ((p & 1) << 6)) + (8 * p + row) * 1280) + 3072 - (i & 3) * 1024; }
        const char* base = wd + (cb16 & 7) * 80 * 1280 + rot * 128 - 3072;
        unsigned lds = 49152 + o * 5120;
        auto issue = [&]() { dma4(vw[0], vw[1], vw[2], vw[3], base, lds); dma1(vw[4], base, lds + 4096); base += --left ? 128 : 128 - 1280; lds = lds >= 49152 + 20480 ? lds - 20480 : lds + 10240; };
        __builtin_amdgcn_s_sleep(WSL);
#pragma unroll 1
        for (int s = 0; s < 3; ++s) issue();
        WAITV_BAR(10);
#pragma unroll 1
        for (int s = 0; s < 7; ++s) { WAITV_BAR(5); issue(); }
        WAITV_BAR(5); WAITV_BAR(0); BAR();
    }
}

DI void retile_wo(const char* wo, rsrc_t rs, int f, int lane) {
    const int cbk = f / 100, st = (f - cbk * 100) / 5, tile = f % 5, kh = st < 16 ? st >> 3 : (st >> 1) & 1, i = st < 16 ? st & 7 : 8 + (st & 1);
    const int rr = lane & 15, g = lane >> 4;
    const int e = cbk * 80 + (tile < 4 ? 32 * ((tile >> 1) ^ kh) + 8 * (rr >> 2) + 4 * (tile & 1) + (rr & 3) : 64 + rr);
    const int k0 = i < 8 ? (16 * kh + 2 * i + (g >> 1)) * 20 + 8 * (g & 1) : (16 * kh + 8 * (i - 8) + 2 * g) * 20 + 16, k4 = i < 8 ? k0 + 4 : k0 + 20;
    const unsigned short* p = (const unsigned short*)wo + e;
    u32x4 v;
    v[0] = p[k0 * 640] | ((unsigned)p[(k0 + 1) * 640] << 16); v[1] = p[(k0 + 2) * 640] | ((unsigned)p[(k0 + 3) * 640] << 16);
    v[2] = p[k4 * 640] | ((unsigned)p[(k4 + 1) * 640] << 16); v[3] = p[(k4 + 2) * 640] | ((unsigned)p[(k4 + 3) * 640] << 16);
    st128(rs, lane * 16, WS_WT + f * 1024, v);
}

DI void seq_compute(rsrc_t rs, int w, int lane, int kq, int mt, int nt, int hid, u64 tag) {
    const unsigned rr = lane & 15, g = lane >> 4;
    unsigned ac[2], ab = 49152 + (w >> 1) * 5120 + lane * 16;
    ac[0] = ((w & 1) * 64 + rr) * 128 + ((g ^ (rr >> 1)) << 4); ac[1] = ac[0] ^ 64;
    PIN(ac[0]); PIN(ac[1]); PIN(ab);
    f32x4 acc[4][5];
#pragma unroll
    for (int j = 0; j < 5; ++j) {
        f32x16 z = zero16(); PIN(z);
#pragma unroll
        for (int i = 0; i < 4; ++i) acc[i][j] = (f32x4){z[4 * i], z[4 * i + 1], z[4 * i + 2], z[4 * i + 3]};
    }
    u32x4 cf[4], af[3];
    WAITVL_BAR();
    if (w == 0) Flag(lane == 0, FW + hid, tag).raise(rs);
    PRIO(0);
#pragma unroll
    for (int i = 0; i < 4; ++i) cf[i] = lds128(ac[0] + i * 2048);
    af[0] = lds128(ab); af[1] = lds128(ab + 1024);
    auto step = [&](int k) {
        const int n = (k + 1) % 6;
#pragma unroll
        for (int j = 0; j < 5; ++j) {
            if (j == 3) WAITL_BAR();
            SBAR();
            af[(k * 5 + j + 2) % 3] = lds128(ab + (j < 3 ? (k % 3) * 10240 + (j + 2) * 1024 : (n % 3) * 10240 + (j - 3) * 1024));
            SBAR();
#pragma unroll
            for (int i0 = 0; i0 < 4; ++i0) {
                const int i = (j & 1) ? 3 - i0 : i0;
                acc[i][j] = mma16(af[(k * 5 + j) % 3], cf[i], acc[i][j]);
                if (j == 4) cf[i] = lds128(ac[n & 1] + (n >> 1) * 16384 + i * 2048);
                SBAR();
            }
        }
    };
#pragma unroll 1
    for (int t = 0; t < 5; ++t) {
        if (__builtin_amdgcn_readfirstlane(t) == GSW) PRIO(1);
#pragma unroll
        for (int k = 0; k < 6; ++k) step(k);
    }
    step(0); step(1);
    PRIO(3);
    const unsigned sb = WS_P + (kq * 256 + (mt * 2 + (w & 1)) * 8 + nt * 2 + (w >> 1)) * 10240;
    auto part = [&](int i) { return i < 8 ? pk44(acc[i >> 1][2 * (i & 1)], acc[i >> 1][2 * (i & 1) + 1]) : pk44(acc[2 * (i & 1)][4], acc[2 * (i & 1) + 1][4]); };
    if (w == kq) {
#pragma unroll
        for (int i = 0; i < 10; ++i) *(LAS u32x4*)(uintptr_t)(59392 + lane * 16 + i * 1024) = part(i);
    } else {
#pragma unroll
        for (int i = 0; i < 10; ++i) st128(rs, lane * 16 + (i & 3) * 1024, sb + (i >> 2) * 4096, part(i));
    }
}

DI void seq_load(const char* c, const char* ws, int li, int lane, int kq, int mt, int nt, int cb, unsigned fw, u64 tag, rsrc_t rs, u32x4 (&wl)[10]) {
    auto frags = [&]() {
#pragma unroll
        for (int i = 0; i < 10; ++i) wl[i] = ld128(rs, lane * 16 + (i & 3) * 1024, WS_WT + (cb * 100 + 90 + (i & ~3)) * 1024);
    };
    const char* wt = ws + WS_WT + cb * 102400;
    const int rot = (nt & 1) * 8;
    if (li < 2) {
        const unsigned row = lane >> 3, v0 = row * 4096 + (((lane & 7) ^ (row >> 1)) << 4), v1 = (v0 ^ 64) + 8 * 4096 - 1024;
        const char* base = c + (size_t)(mt * 128 + 64 * li) * 4096 + (kq & 1) * 2048 + rot * 128;
        unsigned lds = li * 8192; int left = 16 - rot;
        auto next = [&]() { lds = lds >= 32768 ? lds - 32768 : lds + 16384; };
        auto issue = [&]() {
#pragma unroll
            for (int i = 0; i < 4; ++i) { if (NTC) dma2n(v0, v1, base + i * 65536, lds + i * 2048); else dma2(v0, v1, base + i * 65536, lds + i * 2048); }
            base += --left ? 128 : 128 - 2048; next();
        };
#pragma unroll 1
        for (int s = 0; s < 3; ++s) { issue(); if (s == 0) WAITV_BAR(0); }
#pragma unroll 1
        for (int s = 0; s < 13; ++s) { BAR(); WAITV_BAR(8); issue(); }
        frags();
#pragma unroll 1
        for (int s = 0; s < 3; ++s) { BAR(); WAITV_BAR(8); dma4c(lane * 16, wt + lds, lds); dma4c(lane * 16, wt + lds + 4096, lds + 4096); next(); }
    } else {
        const char* bb = ws + WS_AB + (((kq >> 1) * 4 + nt) * 128 + (kq & 1) * 64 + 4 * rot + li - 2) * 5120;
        unsigned lds = 49152 + (li - 2) * 5120; int left = 32 - 2 * rot;
        auto next = [&]() { lds = lds >= 49152 + 20480 ? lds - 20480 : lds + 10240; };
        auto issue = [&]() { dma5c(lane * 16, bb, lds); bb += --left ? 10240 : 10240 - 64 * 5120; next(); };
        await<NA>(ws, FA + ((kq & 1) * 8 + (lane >> 3)) * 16 + (kq >> 1) * 8 + 2 * nt + ((lane >> 2) & 1), tag);
#pragma unroll 1
        for (int s = 0; s < 2; ++s) issue();
        WAITV_BAR(5);
        issue();
#pragma unroll 1
        for (int s = 0; s < 20; ++s) { WAITV_BAR(5); issue(); }
        await(ws, fw, tag);
        frags();
        dma1c(lane * 16, wt + 79872 + (li - 2) * 1024, 79872 + (li - 2) * 1024);
#pragma unroll 1
        for (int s = 20; s < 29; ++s) { WAITV_BAR(5); issue(); }
#pragma unroll 1
        for (int s = 0; s < 2; ++s) { WAITV_BAR(5); dma5c(lane * 16, wt + lds + (s ? 0 : 81920 - 69632), lds); next(); }
        WAITV_BAR(5);
    }
}

DI void role_g(const char* x, const char* cr, const char* ci, const char* wr, const char* wi, const char* wo, char* ws, char* out, rsrc_t rs, int w, int lane, int b) {
    u64 tag = mktag(ldflag(ws, WS_E));
    PINS(tag);
    const int xcd = b & 7, idx = b >> 3;
    const int rb = (xcd & 1) * 8 + (idx & 7), cb16 = (xcd >> 1) * 4 + (idx >> 3), hid = rb * 16 + cb16;
    const int kq = xcd >> 1, mt = (xcd & 1) * 8 + (idx >> 2), nt = idx & 3;
    const int rbk = 2 * mt + (kq & 1), cb = 2 * nt + (kq >> 1);
    if (w < 4) hid_compute(rs, w, lane, rb, cb16);
    else hid_load(x, cb16 < 8 ? wr : wi, ws, w - 4, lane, rb, cb16, b, tag);
    PRIO(3);
    const Flag fa(threadIdx.x == 0, FA + hid, tag);
    WAITVL_BAR();
    fa.raise(rs);
    u32x4 wl[10];
    if (w < 4) {
        if (hid < 200) retile_wo(wo, rs, hid * 4 + w, lane);
        seq_compute(rs, w, lane, kq, mt, nt, hid, tag);
#pragma unroll
        for (int i = 0; i < 10; ++i) wl[i] = lds128(69632 + lane * 16 + i * 1024);
    } else seq_load(kq < 2 ? cr : ci, ws, w - 4, lane, kq, mt, nt, cb, lane < 25 ? FW + 25 * cb + lane : FQ + (rbk >> 1) * 16 + 15, tag, rs, wl);
    const unsigned ts = w & 3, kh = w >> 2, g = lane >> 4, tt = rbk * 2 + (ts >> 1), tq = (ts & 1) * 16 + (lane & 15), l16 = lane * 16;
    const float gate = __uint_as_float(ld32(rs, tq * 4, WS_G + tt * 128));
    const Flag fp(threadIdx.x == 0, FP + (mt * 4 + nt) * 4 + kq, tag);
    WAITVL_BAR();
    fp.raise(rs);
    if (w >= 6) { const unsigned a = 69632 + (w - 6) * 5120; dma5c(l16, ws + WS_WT + cb * 102400 + a, a); }
    u32x4 pa[4]; u32x2 pb[4];
    pa[0] = lds128(59392 + (2 * ts + kh) * 1024 + l16); pb[0] = lds64(59392 + (8 + (ts >> 1)) * 1024 + (ts & 1) * 8 + l16);
    if (w == 4) await<NA>(ws, FP + (mt * 4 + nt) * 4 + (lane >> 4), tag);
    WAITL_BAR();
    if (w >= 6) { const unsigned a = 59392 + (w - 6) * 5120; dma5c(l16, ws + WS_WT + cb * 102400 + a, a); }
    const unsigned ps = WS_P + (rbk * 8 + cb) * 10240;
#pragma unroll
    for (int q = 1; q < 4; ++q) { pa[q] = ld128(rs, l16, ps + (q ^ kq) * (256 * 10240) + (2 * ts + kh) * 1024); pb[q] = ld64(rs, l16, kh ? WS_END : ps + (q ^ kq) * (256 * 10240) + (8 + (ts >> 1)) * 1024 + (ts & 1) * 8); }
    SBAR();
    PRIO(0);
    SBAR();
    float y[12];
#pragma unroll
    for (int e = 0; e < 4; ++e) { y[2 * e] = (bflo(pa[0][e]) + bflo(pa[1][e])) - (bflo(pa[2][e]) + bflo(pa[3][e])); y[2 * e + 1] = (bfhi(pa[0][e]) + bfhi(pa[1][e])) - (bfhi(pa[2][e]) + bfhi(pa[3][e])); }
#pragma unroll
    for (int e = 0; e < 2; ++e) { y[8 + 2 * e] = (bflo(pb[0][e]) + bflo(pb[1][e])) - (bflo(pb[2][e]) + bflo(pb[3][e])); y[9 + 2 * e] = (bfhi(pb[0][e]) + bfhi(pb[1][e])) - (bfhi(pb[2][e]) + bfhi(pb[3][e])); }
#pragma unroll
    for (int i = 0; i < 12; ++i) { y[i] *= kq < 2 ? gate : -gate; PIN(y[i]); }
    float og = 1.f - gate;
    unsigned aw = kh * 40960 + l16, vo = g * 512 + tq * 16, vo2 = g * 512 + tq * 8;
    unsigned oa = (tt * 32 + (ts & 1) * 16 + (lane >> 2)) * 1280 + cb * 160 + kh * 64 + (lane & 3) * 16, ob = kh ? 2048 * 1280 : oa + 128 - (lane & 3) * 8, op = (lane & 3) * 64 + (lane & ~3);
    unsigned o1[4], o2 = WS_O2 + (tt * 32 + 16 * kh) * 256;
#pragma unroll
    for (int i = 0; i < 4; ++i) { o1[i] = WS_O1 + (tt * 32 + 16 * kh) * 1024 + i * 4096; PINS(o1[i]); }
    PIN(og); PIN(oa); PIN(ob); PIN(op); PIN(aw); PIN(vo); PIN(vo2); PINS(o2);
    if (w == 4) await<NO>(ws, FO + (rbk >> 2) * 32 + (lane >> 1), tag);
    WAITV_BAR(0);
    u32x4 of[10];
#pragma unroll
    for (int i = 0; i < 2; ++i) { const u32x2 lo = ld64(rs, vo2 + i * 2048, o2), hi = ld64(rs, vo2 + i * 2048 + 256, o2); of[i] = (u32x4){lo[0], lo[1], hi[0], hi[1]}; }
#pragma unroll
    for (int i = 0; i < 8; ++i) of[2 + i] = ld128(rs, vo + (i & 1) * 2048, o1[i >> 1]);
    SBAR();
    PRIO(3);
    f32x4 acc[5];
#pragma unroll
    for (int n = 0; n < 50; ++n) {
        acc[n % 5] = mma16(wl[n % 10], of[n / 5], n < 5 ? f32x4{} : acc[n % 5]);
        if (n < 40) wl[n % 10] = lds128(aw + n * 1024);
        if (n == 44) BAR();
        SBAR();
    }
    LAS f32x4* mine = (LAS f32x4*)(uintptr_t)(aw + ts * 3072);
    const LAS f32x4* other = (const LAS f32x4*)(uintptr_t)((aw ^ 40960) + ts * 3072);
    mine[0] = acc[2]; mine[64] = acc[3]; mine[128] = acc[4];
    WAITL_BAR();
    const f32x4 a0 = acc[0] + other[0], a1 = acc[1] + other[64], a2 = acc[4] + other[128];
    u32x4 r0 = {pk2(og * a0[0] + y[0], og * a0[1] + y[1]), pk2(og * a0[2] + y[2], og * a0[3] + y[3]), pk2(og * a1[0] + y[4], og * a1[1] + y[5]), pk2(og * a1[2] + y[6], og * a1[3] + y[7])};
    u32x2 r1 = {pk2(og * a2[0] + y[8], og * a2[1] + y[9]), pk2(og * a2[2] + y[10], og * a2[3] + y[11])};
#pragma unroll
    for (int i = 0; i < 4; ++i) r0[i] = __builtin_amdgcn_ds_bpermute(op, r0[i]);
#pragma unroll
    for (int i = 0; i < 2; ++i) r1[i] = __builtin_amdgcn_ds_bpermute(op, r1[i]);
    const rsrc_t ro = __builtin_amdgcn_make_buffer_rsrc(out, 0, 2048 * 1280, 0x00020000);
    __builtin_amdgcn_raw_buffer_store_b128(r0, ro, oa, 0, 0);
    __builtin_amdgcn_raw_buffer_store_b64(r1, ro, ob, 0, 0);
}

__global__ __launch_bounds__(512, 4) void fused_mix(const char* x, const char* cr, const char* ci, const char* wr, const char* wi, const char* wqkv, const char* wo, const char* gw, char* ws, char* out) {
    const int lane = threadIdx.x & 63, w = __builtin_amdgcn_readfirstlane(threadIdx.x >> 6);
    const rsrc_t rs = __builtin_amdgcn_make_buffer_rsrc(ws, 0, WS_END, 0x00020000);
    const int b = blockIdx.x, rb = (b & 1) * 8 + ((b >> 3) & 7), cg = ((b >> 1) & 3) + 4 * (b >> 6);
    if ((b >> 3) == 32 && w < 3) {
        const char* pc; asm volatile("s_getpc_b64 %0" : "=s"(pc));
        const unsigned t = *(const GAS unsigned*)(pc + min((unsigned)w * 8192 + lane * 128, CODE_BYTES));
        asm volatile("" :: "v"(t));
    }
    if (w >= 4 && cg < 15) { PRIO(3); qkv_load(x, wqkv, w - 4, lane, rb, cg); }
    if (b >= 256) { PRIO(2); role_g(x, cr, ci, wr, wi, wo, ws, out, rs, w, lane, b - 256); }
    else { PRIO(3); role_a(x, gw, ws, rs, w, lane, b, rb, cg); }
}

extern "C" void kernel_launch(void* const* d_in, const int* in_sizes, int n_in, void* d_out, int out_size, void* d_ws, size_t ws_size, hipStream_t stream) {
    (void)hipFuncSetAttribute((const void*)fused_mix, hipFuncAttributeMaxDynamicSharedMemorySize, LDS_BYTES);
    fused_mix<<<512, 512, LDS_BYTES, stream>>>((const char*)d_in[0], (const char*)d_in[1], (const char*)d_in[2], (const char*)d_in[3], (const char*)d_in[4],
                                               (const char*)d_in[6], (const char*)d_in[7], (const char*)d_in[8], (char*)d_ws, (char*)d_out);
}
```

```cpp
#include <hip/hip_runtime.h>
#include <hip/hip_bf16.h>
#include <stdint.h>

typedef __attribute__((ext_vector_type(8))) __bf16 bf16x8;
typedef __attribute__((ext_vector_type(2))) __bf16 bf16x2;
typedef __attribute__((ext_vector_type(4))) short s16x4;
typedef __attribute__((ext_vector_type(16))) float f32x16;
typedef __attribute__((ext_vector_type(4))) float f32x4;
typedef __attribute__((ext_vector_type(2))) float f32x2;
typedef __attribute__((ext_vector_type(4))) unsigned u32x4;
typedef __attribute__((ext_vector_type(2))) unsigned u32x2;
typedef unsigned long long u64;
typedef __amdgpu_buffer_rsrc_t rsrc_t;
#define LAS __attribute__((address_space(3)))
#define GAS __attribute__((address_space(1)))
#define DI __device__ __forceinline__
#define SBAR() __builtin_amdgcn_sched_barrier(0)
#define PIN(x) asm volatile("" : "+v"(x))
#define PINS(x) asm volatile("" : "+s"(x))
#define HOLD(t0, us) do { while ((unsigned)__builtin_amdgcn_s_memrealtime() - (t0) < (us) * 100u) __builtin_amdgcn_s_sleep(2); } while (0)
#define PRIO(n) __builtin_amdgcn_s_setprio(n)

constexpr unsigned REC = 3072, KV_HS = 65 * REC, QT = 1536, Q_HS = 64 * QT;
constexpr unsigned WS_KV = 0;
constexpr unsigned WS_Q = WS_KV + 32 * KV_HS;
constexpr unsigned WS_AB = WS_Q + 32 * Q_HS;
constexpr unsigned WS_P = WS_AB + 8 * 64 * 10240;
constexpr unsigned WS_O1 = WS_P + 4 * 256 * 10240;
constexpr unsigned WS_O2 = WS_O1 + 64 * 32 * 1024;
constexpr unsigned WS_WT = WS_O2 + 64 * 32 * 256;
constexpr unsigned WS_G = WS_WT + 800 * 1024;
constexpr unsigned WS_F = WS_G + 8192;
constexpr unsigned WS_E = WS_F + 6 * 256 * 128;
constexpr unsigned WS_END = WS_E + 128;
constexpr unsigned FQ = 0, FA = 256, FP = 512, FW = 768, FL = 1024, FO = 1280;
constexpr unsigned LDS_BYTES = 81920, CODE_BYTES = 24448;
constexpr float QSCALE = 0.22360679774997896f * 1.4426950408889634f;
#define PROBE 0
#define PACE 3
#define AAT 6
#define NL 8
#define NA 8
#define NO 1
#define GSW 2

DI unsigned swap23(unsigned r) { return (r & ~12u) | ((r & 4u) << 1) | ((r & 8u) >> 1); }
DI unsigned pk2(float a, float b) { f32x2 v = {a, b}; return __builtin_bit_cast(unsigned, __builtin_convertvector(v, bf16x2)); }
DI float bflo(unsigned u) { return __uint_as_float(u << 16); }
DI float bfhi(unsigned u) { return __uint_as_float(u & 0xffff0000u); }
DI f32x16 mma32(u32x4 a, u32x4 b, f32x16 c) { return __builtin_amdgcn_mfma_f32_32x32x16_bf16(__builtin_bit_cast(bf16x8, a), __builtin_bit_cast(bf16x8, b), c, 0, 0, 0); }
DI f32x16 mma32k8(u32x2 a, u32x2 b, f32x16 c) { return __builtin_amdgcn_mfma_f32_32x32x8bf16_1k(__builtin_bit_cast(s16x4, a), __builtin_bit_cast(s16x4, b), c, 0, 0, 0); }
DI f32x4 mma16(u32x4 a, u32x4 b, f32x4 c) { return __builtin_amdgcn_mfma_f32_16x16x32_bf16(__builtin_bit_cast(bf16x8, a), __builtin_bit_cast(bf16x8, b), c, 0, 0, 0); }
DI u32x4 pk8(const f32x16& a, int s, float c) { return (u32x4){pk2(a[8 * s] * c, a[8 * s + 1] * c), pk2(a[8 * s + 2] * c, a[8 * s + 3] * c), pk2(a[8 * s + 4] * c, a[8 * s + 5] * c), pk2(a[8 * s + 6] * c, a[8 * s + 7] * c)}; }
DI u32x4 pk8(const f32x16& a, int s) { return (u32x4){pk2(a[8 * s], a[8 * s + 1]), pk2(a[8 * s + 2], a[8 * s + 3]), pk2(a[8 * s + 4], a[8 * s + 5]), pk2(a[8 * s + 6], a[8 * s + 7])}; }
DI u32x4 pk44(f32x4 a, f32x4 b) { return (u32x4){pk2(a[0], a[1]), pk2(a[2], a[3]), pk2(b[0], b[1]), pk2(b[2], b[3])}; }
DI f32x16 zero16() { u32x2 z = {0u, 0u}; PIN(z); return mma32k8(z, z, f32x16{}); }
DI u32x4 lds128(unsigned a) { return *(const LAS u32x4*)(uintptr_t)a; }
DI u32x2 lds64(unsigned a) { return *(const LAS u32x2*)(uintptr_t)a; }
DI u32x2 ldstr(unsigned a) { return __builtin_bit_cast(u32x2, __builtin_amdgcn_ds_read_tr16_b64_v4i16((LAS s16x4*)(uintptr_t)a)); }
DI void st128(rsrc_t rs, unsigned v, unsigned s, u32x4 d) { __builtin_amdgcn_raw_buffer_store_b128(d, rs, v, s, 16); asm volatile("s_nop 1" :: "v"(d) : "memory"); }
DI void st64(rsrc_t rs, unsigned v, unsigned s, u32x2 d) { __builtin_amdgcn_raw_buffer_store_b64(d, rs, v, s, 16); }
DI void st32(rsrc_t rs, unsigned v, unsigned s, unsigned d) { __builtin_amdgcn_raw_buffer_store_b32(d, rs, v, s, 16); }
DI u32x4 ld128(rsrc_t rs, unsigned v, unsigned s) { return __builtin_amdgcn_raw_buffer_load_b128(rs, v, s, 16); }
DI u32x2 ld64(rsrc_t rs, unsigned v, unsigned s) { return __builtin_amdgcn_raw_buffer_load_b64(rs, v, s, 16); }
DI unsigned ld32(rsrc_t rs, unsigned v, unsigned s) { return __builtin_amdgcn_raw_buffer_load_b32(rs, v, s, 16); }
#define M0SET "s_mov_b32 m0, %[l]\n\ts_nop 0\n\t"
#define GLD(v, o, c) "global_load_lds_dwordx4 %[" v "], %[b] offset:" o c "\n\t"
DI void dma1(unsigned v0, const char* base, unsigned lds) { asm volatile(M0SET GLD("v0", "0", "") ::[v0] "v"(v0), [b] "s"(base), [l] "s"(lds) : "memory"); }
DI void dma2(unsigned v0, unsigned v1, const char* base, unsigned lds) { asm volatile(M0SET GLD("v0", "0", "") GLD("v1", "1024", "") ::[v0] "v"(v0), [v1] "v"(v1), [b] "s"(base), [l] "s"(lds) : "memory"); }
DI void dma4(unsigned v0, unsigned v1, unsigned v2, unsigned v3, const char* base, unsigned lds) {
    asm volatile(M0SET GLD("v0", "0", "") GLD("v1", "1024", "") GLD("v2", "2048", "") GLD("v3", "3072", "") ::[v0] "v"(v0), [v1] "v"(v1), [v2] "v"(v2), [v3] "v"(v3), [b] "s"(base), [l] "s"(lds) : "memory");
}
DI void dma1c(unsigned v0, const char* base, unsigned lds) { asm volatile(M0SET GLD("v0", "0", " sc1") ::[v0] "v"(v0), [b] "s"(base), [l] "s"(lds) : "memory"); }
DI void dma4c(unsigned v0, const char* base, unsigned lds) {
    asm volatile(M0SET GLD("v0", "0", " sc1") GLD("v0", "1024", " sc1") GLD("v0", "2048", " sc1") GLD("v0", "3072", " sc1") ::[v0] "v"(v0), [b] "s"(base), [l] "s"(lds) : "memory");
}
DI void dma5c(unsigned v0, const char* base, unsigned lds) { dma4c(v0, base, lds); dma1c(v0, base + 4096, lds + 4096); }
DI void dma_rec(unsigned v0, unsigned v1, const char* base, unsigned lds) {
    asm volatile(M0SET GLD("v0", "0", " sc1") "s_mov_b32 exec_hi, 0xff0fffff\n\t" GLD("v1", "1024", " sc1") "s_mov_b32 exec_lo, 0xffff0fff\n\ts_mov_b32 exec_hi, 0x0fffff0f\n\t" GLD("v0", "2048", " sc1") "s_mov_b64 exec, -1"
                 ::[v0] "v"(v0), [v1] "v"(v1), [b] "s"(base), [l] "s"(lds) : "memory");
}
#define BAR() asm volatile("s_barrier" ::: "memory")
#define WAITV_BAR(N) asm volatile("s_waitcnt vmcnt(" #N ")\n\ts_barrier" ::: "memory")
#define WAITL_BAR() asm volatile("s_waitcnt lgkmcnt(0)\n\ts_barrier" ::: "memory")
#define WAITVL_BAR() asm volatile("s_waitcnt vmcnt(0) lgkmcnt(0)\n\ts_barrier" ::: "memory")

DI u64 ldflag(const char* ws, unsigned off) { return __hip_atomic_load((const GAS u64*)(ws + off), __ATOMIC_RELAXED, __HIP_MEMORY_SCOPE_AGENT); }
DI void stflag(char* ws, unsigned off, u64 v) { __hip_atomic_store((GAS u64*)(ws + off), v, __ATOMIC_RELAXED, __HIP_MEMORY_SCOPE_AGENT); }
DI u64 mktag(u64 e) {
    const u64 u = ((u64)(unsigned)__builtin_amdgcn_readfirstlane((unsigned)(e >> 32)) << 32) | (unsigned)__builtin_amdgcn_readfirstlane((unsigned)e);
    return u * 6364136223846793005ull + 1442695040888963407ull;
}
struct Flag {
    unsigned v; u32x2 d;
    DI Flag(bool mine, unsigned f, u64 tag) { v = mine ? WS_F + f * 128 : WS_END; d = (u32x2){(unsigned)tag, (unsigned)(tag >> 32)}; PIN(v); PIN(d); }
    DI void raise(rsrc_t rs) const { st64(rs, v, 0, d); }
};
template <int NAP = 1> DI void await(const char* ws, unsigned f0, u64 tag) {
    u64 a; unsigned n = 1u << 19;
    asm volatile("1:\n\tglobal_load_dwordx2 %0, %2, %3 sc1\n\ts_sub_u32 %1, %1, 1\n\ts_sleep %c5\n\ts_waitcnt vmcnt(0)\n\tv_cmp_ne_u64 vcc, %4, %0\n\ts_cbranch_scc1 2f\n\ts_cbranch_vccnz 1b\n2:"
                 : "=&v"(a), "+s"(n) : "v"(WS_F + f0 * 128), "s"(ws), "s"(tag), "i"(NAP) : "vcc", "scc", "memory");
}
DI void await(const char* ws, unsigned f0, unsigned f1, u64 tag) {
    u64 a, b, m; unsigned n = 1u << 19;
    asm volatile("1:\n\tglobal_load_dwordx2 %0, %4, %6 sc1\n\tglobal_load_dwordx2 %1, %5, %6 sc1\n\ts_sub_u32 %3, %3, 1\n\ts_cbranch_scc1 2f\n\ts_waitcnt vmcnt(0)\n\t"
                 "v_cmp_ne_u64 %2, %7, %0\n\tv_cmp_ne_u64 vcc, %7, %1\n\ts_or_b64 vcc, vcc, %2\n\ts_cbranch_scc1 1b\n2:\n\ts_waitcnt vmcnt(0)"
                 : "=&v"(a), "=&v"(b), "=&s"(m), "+s"(n) : "v"(WS_F + f0 * 128), "v"(WS_F + f1 * 128), "s"(ws), "s"(tag) : "vcc", "scc", "memory");
}

template <int T>
DI void qkv_compute(rsrc_t rs, int w, int lane, int rb, int cg, int b, u64 ep, unsigned t0) {
    const unsigned r = lane & 31, hh = lane >> 5, wm = w & 1, wn = w >> 1, q = (lane & 15) >> 2, p = lane & 3;
    const unsigned pp = T == 0 ? ((p & 1) << 1) | (p >> 1) : p;
    unsigned xa = (64 * wm + r) * 128 + ((hh ^ ((r >> 1) & 7)) << 4), wa[2];
    wa[0] = 49152 + (8 * hh + q) * 256 + (((2 * wn) ^ q) << 6) + ((lane >> 4) & 1) * 32 + pp * 8; wa[1] = wa[0] ^ 64;
    PIN(xa); PIN(wa[0]); PIN(wa[1]);
    const unsigned kt = rb * 4 + 2 * wm;
    const bool isk = cg >= 5;
    unsigned va[4], sb, ts = REC;
    if (T == 0) {
        const unsigned ct = isk ? cg - 5 : cg, hs = isk ? KV_HS : Q_HS, slot = isk ? r : swap23(r);
        ts = isk ? REC : QT; sb = (isk ? WS_KV : WS_Q) + kt * ts;
#pragma unroll
        for (int i = 0; i < 4; ++i) { const unsigned oc = ct * 16 + 8 * wn + 2 * i + hh, hp = oc / 5, ty = oc - hp * 5; va[i] = (2 * hp + (ty > 2)) * hs + (ty > 2 ? ty - 3 : ty) * 512 + slot * 16; PIN(va[i]); }
    } else {
        sb = WS_KV + kt * REC + 1536;
#pragma unroll
        for (int i = 0; i < 2; ++i) { const unsigned c = (cg - 10) * 128 + 64 * wn + 32 * i + r, head = c / 20, d = c - head * 20; va[i] = head * KV_HS + hh * 384 + (d < 16 ? swap23(d) : d) * 16; PIN(va[i]); }
    }
    f32x16 acc[2][2];
#pragma unroll
    for (int i = 0; i < 4; ++i) { acc[i >> 1][i & 1] = zero16(); PIN(acc[i >> 1][i & 1]); }
    u32x4 xf[2][2], wf[2][2];
    auto rd_x = [&](int set, int g, int mi) { xf[set][mi] = lds128((xa ^ ((g & 3) << 5)) + mi * 4096); };
    auto rd_w = [&](int set, int g, int ni) {
        const unsigned a = wa[ni] + (g & 7) * 4096;
        const u32x2 lo = ldstr(a), hi = ldstr(a + 1024); wf[set][ni] = (u32x4){lo[0], lo[1], hi[0], hi[1]};
    };
    WAITL_BAR();
    if (PROBE == 1) HOLD(t0, 10);
    rd_x(0, 0, 0); rd_w(0, 0, 0); rd_x(0, 0, 1); rd_w(0, 0, 1);
    unsigned xs = 0;
#pragma unroll 1
    for (int t = 0; t < 5; ++t) {
        if (PROBE == 2 && t == PACE) HOLD(t0, 15);
        if (__builtin_expect(t == PACE && w == 0, 0)) Flag(lane == 0, FL + b, mktag(ep)).raise(rs);
#pragma unroll
        for (int g = 0; g < 8; ++g) {
            const int c = g & 1, n = c ^ 1;
            if (c) WAITL_BAR();
            if ((g & 3) == 3) { const unsigned d = xs == 32768 ? -32768 : 16384; xs += d; xa += d; }
            SBAR();
#pragma unroll
            for (int i = 0; i < 4; ++i) {
                const int mi = i & 1, ni = i >> 1;
                acc[mi][ni] = T == 0 ? mma32(wf[c][ni], xf[c][mi], acc[mi][ni]) : mma32(xf[c][mi], wf[c][ni], acc[mi][ni]);
                if (i == 0) { rd_x(n, g + 1, 0); rd_w(n, g + 1, 0); }
                if (i == 1) { rd_x(n, g + 1, 1); rd_w(n, g + 1, 1); }
                SBAR();
            }
        }
    }
    const float sc = isk ? 1.f : QSCALE;
#pragma unroll
    for (int i = 0; i < 8; ++i) {
        const int mi = (i >> 1) & 1, ni = i >> 2, s = i & 1;
        if (T == 0) st128(rs, va[2 * ni + s], sb + mi * ts, pk8(acc[mi][ni], s, sc));
        else st128(rs, va[ni], sb + (mi + s) * REC + s * 768, pk8(acc[mi][ni], s));
    }
}

DI void qkv_load(const char* x, const char* wqkv, int li, int lane, int rb, int cg) {
    const int rot = 5 * ((rb ^ (cg >> 2)) & 1);
    if (li >= 2) {
        unsigned v0 = (lane >> 4) * 3840 + ((((lane >> 2) & 3) ^ (lane >> 4)) << 6) + (lane & 3) * 16, v1 = v0 + 14336, v2 = v0 + 2 * 14336, v3 = v0 + 3 * 14336;
        unsigned off = cg * 256 + (16 * (li - 2) + 64 * rot) * 3840, lds = 49152 + (li - 2) * 4096;
        PIN(v0); PIN(v1); PIN(v2); PIN(v3); PINS(off); PINS(lds); SBAR();
        const char* base = wqkv + off;
        unsigned slot = 0; int left = 20 - 2 * rot;
        auto issue = [&]() { dma4(v0, v1, v2, v3, base, lds + slot); base += --left ? 32 * 3840 : (32 - 640) * 3840; slot = (slot + 8192) & 32767; };
        issue(); issue();
        WAITV_BAR(4);
        issue();
#pragma unroll 1
        for (int s = 0; s < 17; ++s) { issue(); WAITV_BAR(8); }
        WAITV_BAR(4); WAITV_BAR(0); BAR();
    } else {
        const unsigned row = lane >> 3;
        unsigned v0 = swap23(row) * 1280 + (((lane & 7) ^ (row >> 1)) << 4), v1 = (v0 ^ 64) + 4 * 1280 - 1024;
        unsigned off = (rb * 128 + 64 * li) * 1280 + rot * 128, lds = li * 8192;
        PIN(v0); PIN(v1); PINS(off); PINS(lds); SBAR();
        const char* base = x + off;
        int left = 10 - rot;
        auto issue = [&]() {
#pragma unroll
            for (int i = 0; i < 4; ++i) dma2(v0, v1, base + i * 16 * 1280, lds + i * 2048);
            base += --left ? 128 : 128 - 1280; lds = lds >= 32768 ? lds - 32768 : lds + 16384;
        };
#pragma unroll 1
        for (int s = 0; s < 2; ++s) issue();
        WAITV_BAR(8);
#pragma unroll 1
        for (int s = 0; s < 8; ++s) { issue(); BAR(); WAITV_BAR(8); }
        BAR(); WAITV_BAR(0); BAR(); BAR();
    }
}

DI void gate_rows(const char* x, const char* gw, rsrc_t rs, int rb) {
    const int row = rb * 128 + (threadIdx.x >> 2), sub = threadIdx.x & 3;
    float s = 0.f;
#pragma unroll 5
    for (int i = 0; i < 20; ++i) {
        const u32x4 a = *(const u32x4*)(x + row * 1280 + (4 * i + sub) * 16), g = *(const u32x4*)(gw + (4 * i + sub) * 16);
#pragma unroll
        for (int e = 0; e < 4; ++e) s += bflo(a[e]) * bflo(g[e]) + bfhi(a[e]) * bfhi(g[e]);
    }
    s += __shfl_xor(s, 1); s += __shfl_xor(s, 2);
    if (sub == 0) st32(rs, row * 4, WS_G, __float_as_uint(1.f / (1.f + __expf(-s))));
}

DI float rowmax16(const f32x16& s) {
    float m = fmaxf(fmaxf(s[0], s[1]), s[2]);
#pragma unroll
    for (int i = 3; i < 15; i += 2) m = fmaxf(fmaxf(m, s[i]), s[i + 1]);
    return fmaxf(m, s[15]);
}
DI float halfmax(float m) { const auto t = __builtin_amdgcn_permlane32_swap(__float_as_uint(m), __float_as_uint(m), false, false); return fmaxf(__uint_as_float(t[0]), __uint_as_float(t[1])); }
DI float upper(float m) { const auto t = __builtin_amdgcn_permlane32_swap(__float_as_uint(m), __float_as_uint(m), false, false); return __uint_as_float(t[1]); }

DI void attention(char* ws, rsrc_t rs, int w, int lane, int head, int qb, u64 tag, unsigned t0) {
    const unsigned r = lane & 31, hh = lane >> 5, kh = w >> 2, qt = qb * 8 + 2 * (w & 3), odd = head & 1;
    const unsigned kv = WS_KV + head * KV_HS, kv2 = kv - odd * KV_HS + 1024;
    unsigned l16 = lane * 16, l8 = hh ? WS_END : r * 16 + odd * 8, d0 = l16 + KV_HS, d1 = hh ? d0 : d0 - odd * KV_HS;
    unsigned vg = r < 20 ? 1536 + hh * 384 + r * 16 : WS_END, vfill = r == 20 ? 0x3f803f80u : 0u, kone = hh ? 0x3f80u : 0u;
    const char* src = ws + kv + (32 * kh + 1 + (w & 3)) * REC - KV_HS;
    unsigned dst = w * REC, kvh = kv + 32 * kh * REC, kv2h = kv2 + 32 * kh * REC;
    unsigned qa = WS_Q + head * Q_HS + (qt + 1 - kh) * QT, qa2 = qa + 1024 - odd * Q_HS;
    unsigned o1 = WS_O1 + ((qt + kh) * 32 + head) * 1024, o2 = WS_O2 + ((qt + kh) * 32 + head) * 256;
    const unsigned lb = kh * 4 * REC;
    unsigned ak1 = lb + l16, ak2 = lb + (hh ? 1536 + 22 * 16 : 1024 + r * 16 + odd * 8), av = lb + 1536 + (hh ^ (r > 27)) * 384 + min(r, 21u) * 16;
    unsigned xm = 57344 + w * 3072 + l16, xo = 57344 + (w ^ 4) * 3072 + l16;
    u32x4 p0, p1 = u32x4{}, v1 = u32x4{};
    f32x16 oa = zero16(), ob = zero16();
    float e[16]; e[12] = 0.f; e[13] = 0.f; e[14] = 0.f; e[15] = 0.f;
    PIN(l16); PIN(l8); PIN(d0); PIN(d1); PIN(vg); PIN(vfill); PIN(kone); PIN(ak1); PIN(ak2); PIN(av); PIN(xm); PIN(xo);
    PIN(p1); PIN(v1); PIN(oa); PIN(ob); PIN(e[12]); PIN(e[13]); PIN(e[14]); PIN(e[15]);
    PINS(src); PINS(dst); PINS(kvh); PINS(kv2h); PINS(qa); PINS(qa2); PINS(o1); PINS(o2);
    if (w == 4) {
        const unsigned c0 = (20 * head) >> 7, c1 = (20 * head + 19) >> 7;
        await(ws, FQ + ((lane >> 1) & 15) * 16 + 5 + 5 * (lane >> 5) + ((lane & 1) ? c1 : c0), FQ + (2 * qb + (lane >> 5)) * 16 + ((lane & 16) ? c1 : c0), tag);
    }
    BAR();
    if (PROBE == 4) HOLD(t0, 30);
    u32x2 k2 = ld64(rs, l8, kv2h), q2a = ld64(rs, l8, qa2);
    u32x4 k1 = ld128(rs, l16, kvh);
    const u32x4 q1a = ld128(rs, l16, qa);
    SBAR();
    u32x4 v0 = ld128(rs, vg, kvh);
    dma_rec(d0, d1, src, dst);
    SBAR();
    f32x16 sa = mma32(k1, q1a, mma32k8(k2, q2a, f32x16{})), sb;
    *(LAS u32x4*)(uintptr_t)(xm + 1024) = q1a;
    unsigned na = pk2(-halfmax(rowmax16(sa)), 0.f); PIN(na);
    q2a[0] = hh ? na : q2a[0]; k2[0] |= kone;
    *(LAS u32x2*)(uintptr_t)xm = q2a;
    sa = mma32(k1, q1a, mma32k8(k2, q2a, f32x16{}));
    SBAR();
    WAITL_BAR();
    u32x2 q2b = lds64(xo);
    const u32x4 q1b = lds128(xo + 1024);
    v0 |= vfill;
    const int jl = kh ? 7 : 0;
#define EX(C, i) do { e[i] = __builtin_amdgcn_exp2f(C[i]); PIN(e[i]); SBAR(); } while (0)
#define CV(P, n, i) do { unsigned t_ = pk2(e[i], e[i + 1]); PIN(t_); P[n] = t_; SBAR(); } while (0)
#define UNIT(C, N, O, OP, Q1, Q2, u)                                                                          \
    do {                                                                                                      \
        SBAR();                                                                                               \
        N = mma32k8(k2, Q2, f32x16{}); SBAR();                                                                \
        if (u == AAT) PRIO(0);                                                                                \
        if (u == 0) {                                                                                         \
            WAITVL_BAR(); PRIO(2);                                                                            \
            if (__builtin_expect(j + jj < jl, 0)) { src += 4 * REC; dma_rec(d0, d1, src - 32 * REC, dst - 4 * REC + 32768 - fl); dma_rec(d0, d1, src, dst + 32768 - fl); } \
        }                                                                                                     \
        if (!(u & 1)) { k2 = lds64(ak2 + (u >> 1) * REC + fl); SBAR(); }                                      \
        EX(C, 0); CV(p1, 2, 12); EX(C, 1); CV(p1, 3, 14); EX(C, 2); EX(C, 3);                                 \
        OP = mma32(v1, p1, OP); SBAR();                                                                       \
        if (!(u & 1)) { v1 = lds128(av + 768 + (u >> 1) * REC + fl); SBAR(); }                                \
        EX(C, 4); CV(p0, 0, 0); EX(C, 5); CV(p0, 1, 2); EX(C, 6); EX(C, 7);                                   \
        N = mma32(k1, Q1, N); SBAR();                                                                         \
        if (!(u & 1)) { k1 = lds128(ak1 + (u >> 1) * REC + fl); SBAR(); }                                     \
        EX(C, 8); CV(p0, 2, 4); EX(C, 9); CV(p0, 3, 6); EX(C, 10); EX(C, 11);                                 \
        O = mma32(v0, p0, O); SBAR();                                                                         \
        if (u & 1) { v0 = lds128(av + (u >> 1) * REC + fl); SBAR(); }                                         \
        EX(C, 12); CV(p1, 0, 8); EX(C, 13); CV(p1, 1, 10); EX(C, 14); EX(C, 15);                              \
    } while (0)
#pragma unroll 1
    for (int j = 0; j < 8; j += 2) {
#pragma unroll
        for (int jj = 0; jj < 2; ++jj) {
            const unsigned fl = jj * 32768;
            UNIT(sa, sb, oa, ob, q1b, q2b, 0); UNIT(sb, sa, ob, oa, q1a, q2a, 1); UNIT(sa, sb, oa, ob, q1b, q2b, 2); UNIT(sb, sa, ob, oa, q1a, q2a, 3);
            UNIT(sa, sb, oa, ob, q1b, q2b, 4); UNIT(sb, sa, ob, oa, q1a, q2a, 5); UNIT(sa, sb, oa, ob, q1b, q2b, 6); UNIT(sb, sa, ob, oa, q1a, q2a, 7);
        }
    }
#undef UNIT
#undef EX
#undef CV
    LAS f32x4* mine = (LAS f32x4*)(uintptr_t)xm;
    const LAS f32x4* other = (const LAS f32x4*)(uintptr_t)xo;
    p1[2] = pk2(e[12], e[13]); p1[3] = pk2(e[14], e[15]);
    ob = mma32(v1, p1, ob);
#pragma unroll
    for (int i = 0; i < 3; ++i) mine[64 * i] = (f32x4){oa[4 * i], oa[4 * i + 1], oa[4 * i + 2], oa[4 * i + 3]};
    WAITL_BAR();
    PRIO(3);
    f32x16 o;
#pragma unroll
    for (int i = 0; i < 3; ++i) {
        const f32x4 t = other[64 * i];
#pragma unroll
        for (int c = 0; c < 4; ++c) o[4 * i + c] = ob[4 * i + c] + t[c];
    }
    float l = upper(o[8]);
    const float mag = ((fabsf(o[0]) + fabsf(o[1])) + (fabsf(o[2]) + fabsf(o[3]))) + ((fabsf(o[4]) + fabsf(o[5])) + (fabsf(o[6]) + fabsf(o[7]))) + ((fabsf(o[8]) + fabsf(o[9])) + (fabsf(o[10]) + fabsf(o[11])));
    if (__builtin_expect(__any(!(mag * 0x1p-20f + l < 0x1p100f) || !(l > 0x1p-40f)), 0)) {
        if (hh) q2b[0] = 0u;
        float m = -INFINITY;
#pragma unroll 1
        for (int t = 0; t < 64; ++t) m = fmaxf(m, rowmax16(mma32(ld128(rs, l16, kv + t * REC), q1b, mma32k8(ld64(rs, l8, kv2 + t * REC), q2b, f32x16{}))));
        m = halfmax(m);
        o = f32x16{};
#pragma unroll 1
        for (int t = 0; t < 64; ++t) {
            const u32x4 va = ld128(rs, vg, kv + t * REC) | vfill, vb = ld128(rs, vg, kv + (t + 1) * REC + 768) | vfill;
            f32x16 s = mma32(ld128(rs, l16, kv + t * REC), q1b, mma32k8(ld64(rs, l8, kv2 + t * REC), q2b, f32x16{}));
#pragma unroll
            for (int i = 0; i < 16; ++i) s[i] = __builtin_amdgcn_exp2f(s[i] - m);
            o = mma32(vb, pk8(s, 1), mma32(va, pk8(s, 0), o));
        }
        l = upper(o[8]);
    }
    const float sc = __builtin_amdgcn_rcpf(l);
    st128(rs, l16, o1, pk8(o, 0, sc));
    st64(rs, hh ? WS_END : l16 >> 1, o2, (u32x2){pk2(o[8] * sc, o[9] * sc), pk2(o[10] * sc, o[11] * sc)});
}

DI void role_a(const char* x, const char* gw, char* ws, rsrc_t rs, int w, int lane, int b, int rb, int cg, unsigned t0) {
    const int head = (b & 7) * 4 + ((b >> 3) & 3), qb = b >> 5;
    const u64 ep = ldflag(ws, WS_E);
    if (__builtin_expect(cg == 15, 0)) { gate_rows(x, gw, rs, rb); Flag(threadIdx.x == 0, FL + b, mktag(ep)).raise(rs); }
    if (w >= 4) {
        const unsigned i = threadIdx.x - 256, rec = i >> 4, c = (i & 3) == 0 ? 0x3f803f80u : 0u;
        *(LAS u32x4*)(uintptr_t)((rec >> 3) * 32768 + (rec & 7) * REC + 1536 + ((i >> 2) & 3) * 384 + 320 + (i & 3) * 16) = (u32x4){(i & 3) == 2 ? 0x3f80u : c, c, c, c};
        const Flag fq(threadIdx.x == 320, FQ + rb * 16 + cg, mktag(ep));
        WAITVL_BAR();
        fq.raise(rs);
    } else {
        if (cg < 10) qkv_compute<0>(rs, w, lane, rb, cg, b, ep, t0); else if (cg < 15) qkv_compute<1>(rs, w, lane, rb, cg, b, ep, t0);
        WAITV_BAR(0);
    }
    const u64 tag = mktag(ep);
    attention(ws, rs, w, lane, head, qb, tag, t0);
    const Flag fo(threadIdx.x == 0, FO + qb * 32 + head, tag);
    WAITV_BAR(0);
    fo.raise(rs);
    if (b == 0) {
        await(ws, FQ + threadIdx.x, tag);
        BAR();
        if (threadIdx.x == 0) stflag(ws, WS_E, tag);
    }
}

DI void hid_compute(rsrc_t rs, int w, int lane, int rb, int cb16) {
    const unsigned rr = lane & 15, g = lane >> 4, rel = rr * 128 + ((g ^ (rr >> 1)) << 4);
    unsigned xa = w * 4096 + rel, wa = 49152 + rel;
    f32x4 acc[2][5];
#pragma unroll
    for (int i = 0; i < 10; ++i) { acc[i / 5][i % 5] = f32x4{}; PIN(acc[i / 5][i % 5]); }
    PIN(xa); PIN(wa);
    u32x4 f[2][7];
    auto rd = [&](int set, int ks, int i) { f[set][i] = i < 2 ? lds128((xa ^ (ks << 6)) + i * 2048) : lds128((wa ^ (ks << 6)) + (i - 2) * 2048); };
    WAITL_BAR();
    PRIO(3);
#pragma unroll
    for (int i = 0; i < 7; ++i) rd(0, 0, i);
    unsigned sb = 0;
#pragma unroll 2
    for (int s = 0; s < 10; ++s) {
#pragma unroll
        for (int ks = 0; ks < 2; ++ks) {
            if (ks) { WAITL_BAR(); const int d = sb == 2 ? -2 : 1; sb += d; xa += d * 16384; wa += d * 10240; }
            SBAR();
#pragma unroll
            for (int i = 0; i < 10; ++i) { acc[i & 1][i >> 1] = mma16(f[ks][i & 1], f[ks][2 + (i >> 1)], acc[i & 1][i >> 1]); if (i < 7) rd(ks ^ 1, ks ^ 1, i); SBAR(); }
        }
    }
    const unsigned so = WS_AB + ((cb16 >> 1) * 64 + rb * 4 + w) * 10240 + (cb16 & 1) * 5120;
#pragma unroll
    for (int j = 0; j < 5; ++j) st128(rs, lane * 16 + (j & 3) * 1024, so + (j >> 2) * 4096, pk44(acc[0][j], acc[1][j]));
}

DI void hid_load(const char* x, const char* wd, const char* ws, int li, int lane, int rb, int cb16, int b, u64 tag) {
    const int rot = ((rb & 7) * 5) >> 2;
    const unsigned row = lane >> 3, ev = swap23(row) * 1280 + (((lane & 7) ^ (row >> 1)) << 4);
    int left = 10 - rot;
    if (li < 2) {
        const unsigned v1 = (ev ^ 64) + 16 * 1280 - 1024;
        const char* base = x + (rb * 128 + 64 * li) * 1280 + rot * 128;
        unsigned lds = li * 8192;
        auto issue = [&]() {
#pragma unroll
            for (int i = 0; i < 4; ++i) dma2(ev, v1, base + ((i >> 1) * 32 + (i & 1) * 4) * 1280, lds + i * 2048);
            base += --left ? 128 : 128 - 1280; lds = lds >= 32768 ? lds - 32768 : lds + 16384;
        };
        await<NL>(ws, FL + b, tag);
#pragma unroll 1
        for (int s = 0; s < 3; ++s) { issue(); if (s == 1) WAITV_BAR(8); }
#pragma unroll 1
        for (int s = 0; s < 7; ++s) { WAITV_BAR(8); issue(); }
        WAITV_BAR(8); WAITV_BAR(0); BAR();
    } else {
        const unsigned o = li - 2;
        unsigned vw[5];
#pragma unroll
        for (int i = 0; i < 5; ++i) { const unsigned p = 5 * o + i; vw[i] = (p < 8 ? (ev ^ ((p & 1) << 6)) + (32 * (p >> 2) + 4 * ((p >> 1) & 1) + 16 * (p & 1)) * 1280 : ((ev & 127) ^ ((p & 1) << 6)) + (8 * p + row) * 1280) + 3072 - (i & 3) * 1024; }
        const char* base = wd + (cb16 & 7) * 80 * 1280 + rot * 128 - 3072;
        unsigned lds = 49152 + o * 5120;
        auto issue = [&]() { dma4(vw[0], vw[1], vw[2], vw[3], base, lds); dma1(vw[4], base, lds + 4096); base += --left ? 128 : 128 - 1280; lds = lds >= 49152 + 20480 ? lds - 20480 : lds + 10240; };
        __builtin_amdgcn_s_sleep(32);
#pragma unroll 1
        for (int s = 0; s < 3; ++s) issue();
        WAITV_BAR(10);
#pragma unroll 1
        for (int s = 0; s < 7; ++s) { WAITV_BAR(5); issue(); }
        WAITV_BAR(5); WAITV_BAR(0); BAR();
    }
}

DI void retile_wo(const char* wo, rsrc_t rs, int f, int lane) {
    const int cbk = f / 100, st = (f - cbk * 100) / 5, tile = f % 5, kh = st < 16 ? st >> 3 : (st >> 1) & 1, i = st < 16 ? st & 7 : 8 + (st & 1);
    const int rr = lane & 15, g = lane >> 4;
    const int e = cbk * 80 + (tile < 4 ? 32 * ((tile >> 1) ^ kh) + 8 * (rr >> 2) + 4 * (tile & 1) + (rr & 3) : 64 + rr);
    const int k0 = i < 8 ? (16 * kh + 2 * i + (g >> 1)) * 20 + 8 * (g & 1) : (16 * kh + 8 * (i - 8) + 2 * g) * 20 + 16, k4 = i < 8 ? k0 + 4 : k0 + 20;
    const unsigned short* p = (const unsigned short*)wo + e;
    u32x4 v;
    v[0] = p[k0 * 640] | ((unsigned)p[(k0 + 1) * 640] << 16); v[1] = p[(k0 + 2) * 640] | ((unsigned)p[(k0 + 3) * 640] << 16);
    v[2] = p[k4 * 640] | ((unsigned)p[(k4 + 1) * 640] << 16); v[3] = p[(k4 + 2) * 640] | ((unsigned)p[(k4 + 3) * 640] << 16);
    st128(rs, lane * 16, WS_WT + f * 1024, v);
}

DI void seq_compute(rsrc_t rs, int w, int lane, int kq, int mt, int nt, int hid, u64 tag) {
    const unsigned rr = lane & 15, g = lane >> 4;
    unsigned ac[2], ab = 49152 + (w >> 1) * 5120 + lane * 16;
    ac[0] = ((w & 1) * 64 + rr) * 128 + ((g ^ (rr >> 1)) << 4); ac[1] = ac[0] ^ 64;
    PIN(ac[0]); PIN(ac[1]); PIN(ab);
    f32x4 acc[4][5];
#pragma unroll
    for (int j = 0; j < 5; ++j) {
        f32x16 z = zero16(); PIN(z);
#pragma unroll
        for (int i = 0; i < 4; ++i) acc[i][j] = (f32x4){z[4 * i], z[4 * i + 1], z[4 * i + 2], z[4 * i + 3]};
    }
    u32x4 cf[4], af[3];
    WAITVL_BAR();
    if (w == 0) Flag(lane == 0, FW + hid, tag).raise(rs);
    PRIO(0);
#pragma unroll
    for (int i = 0; i < 4; ++i) cf[i] = lds128(ac[0] + i * 2048);
    af[0] = lds128(ab); af[1] = lds128(ab + 1024);
    auto step = [&](int k) {
        const int n = (k + 1) % 6;
#pragma unroll
        for (int j = 0; j < 5; ++j) {
            if (j == 3) WAITL_BAR();
            SBAR();
            af[(k * 5 + j + 2) % 3] = lds128(ab + (j < 3 ? (k % 3) * 10240 + (j + 2) * 1024 : (n % 3) * 10240 + (j - 3) * 1024));
            SBAR();
#pragma unroll
            for (int i0 = 0; i0 < 4; ++i0) {
                const int i = (j & 1) ? 3 - i0 : i0;
                acc[i][j] = mma16(af[(k * 5 + j) % 3], cf[i], acc[i][j]);
                if (j == 4) cf[i] = lds128(ac[n & 1] + (n >> 1) * 16384 + i * 2048);
                SBAR();
            }
        }
    };
#pragma unroll 1
    for (int t = 0; t < 5; ++t) {
        if (__builtin_amdgcn_readfirstlane(t) == GSW) PRIO(1);
#pragma unroll
        for (int k = 0; k < 6; ++k) step(k);
    }
    step(0); step(1);
    PRIO(3);
    const unsigned sb = WS_P + (kq * 256 + (mt * 2 + (w & 1)) * 8 + nt * 2 + (w >> 1)) * 10240;
    auto part = [&](int i) { return i < 8 ? pk44(acc[i >> 1][2 * (i & 1)], acc[i >> 1][2 * (i & 1) + 1]) : pk44(acc[2 * (i & 1)][4], acc[2 * (i & 1) + 1][4]); };
    if (w == kq) {
#pragma unroll
        for (int i = 0; i < 10; ++i) *(LAS u32x4*)(uintptr_t)(59392 + lane * 16 + i * 1024) = part(i);
    } else {
#pragma unroll
        for (int i = 0; i < 10; ++i) st128(rs, lane * 16 + (i & 3) * 1024, sb + (i >> 2) * 4096, part(i));
    }
}

DI void seq_load(const char* c, const char* ws, int li, int lane, int kq, int mt, int nt, int cb, unsigned fw, u64 tag, rsrc_t rs, u32x4 (&wl)[10]) {
    auto frags = [&]() {
#pragma unroll
        for (int i = 0; i < 10; ++i) wl[i] = ld128(rs, lane * 16 + (i & 3) * 1024, WS_WT + (cb * 100 + 90 + (i & ~3)) * 1024);
    };
    const char* wt = ws + WS_WT + cb * 102400;
    const int rot = (nt & 1) * 8;
    if (li < 2) {
        const unsigned row = lane >> 3, v0 = row * 4096 + (((lane & 7) ^ (row >> 1)) << 4), v1 = (v0 ^ 64) + 8 * 4096 - 1024;
        const char* base = c + (size_t)(mt * 128 + 64 * li) * 4096 + (kq & 1) * 2048 + rot * 128;
        unsigned lds = li * 8192; int left = 16 - rot;
        auto next = [&]() { lds = lds >= 32768 ? lds - 32768 : lds + 16384; };
        auto issue = [&]() {
#pragma unroll
            for (int i = 0; i < 4; ++i) dma2(v0, v1, base + i * 65536, lds + i * 2048);
            base += --left ? 128 : 128 - 2048; next();
        };
#pragma unroll 1
        for (int s = 0; s < 3; ++s) { issue(); if (s == 0) WAITV_BAR(0); }
#pragma unroll 1
        for (int s = 0; s < 13; ++s) { BAR(); WAITV_BAR(8); issue(); }
        frags();
#pragma unroll 1
        for (int s = 0; s < 3; ++s) { BAR(); WAITV_BAR(8); dma4c(lane * 16, wt + lds, lds); dma4c(lane * 16, wt + lds + 4096, lds + 4096); next(); }
    } else {
        const char* bb = ws + WS_AB + (((kq >> 1) * 4 + nt) * 128 + (kq & 1) * 64 + 4 * rot + li - 2) * 5120;
        unsigned lds = 49152 + (li - 2) * 5120; int left = 32 - 2 * rot;
        auto next = [&]() { lds = lds >= 49152 + 20480 ? lds - 20480 : lds + 10240; };
        auto issue = [&]() { dma5c(lane * 16, bb, lds); bb += --left ? 10240 : 10240 - 64 * 5120; next(); };
        await<NA>(ws, FA + ((kq & 1) * 8 + (lane >> 3)) * 16 + (kq >> 1) * 8 + 2 * nt + ((lane >> 2) & 1), tag);
#pragma unroll 1
        for (int s = 0; s < 2; ++s) issue();
        WAITV_BAR(5);
        issue();
#pragma unroll 1
        for (int s = 0; s < 20; ++s) { WAITV_BAR(5); issue(); }
        await(ws, fw, tag);
        frags();
        dma1c(lane * 16, wt + 79872 + (li - 2) * 1024, 79872 + (li - 2) * 1024);
#pragma unroll 1
        for (int s = 20; s < 29; ++s) { WAITV_BAR(5); issue(); }
#pragma unroll 1
        for (int s = 0; s < 2; ++s) { WAITV_BAR(5); dma5c(lane * 16, wt + lds + (s ? 0 : 81920 - 69632), lds); next(); }
        WAITV_BAR(5);
    }
}

DI void role_g(const char* x, const char* cr, const char* ci, const char* wr, const char* wi, const char* wo, char* ws, char* out, rsrc_t rs, int w, int lane, int b, unsigned t0) {
    u64 tag = mktag(ldflag(ws, WS_E));
    PINS(tag);
    const int xcd = b & 7, idx = b >> 3;
    const int rb = (xcd & 1) * 8 + (idx & 7), cb16 = (xcd >> 1) * 4 + (idx >> 3), hid = rb * 16 + cb16;
    const int kq = xcd >> 1, mt = (xcd & 1) * 8 + (idx >> 2), nt = idx & 3;
    const int rbk = 2 * mt + (kq & 1), cb = 2 * nt + (kq >> 1);
    if (w < 4) hid_compute(rs, w, lane, rb, cb16);
    else hid_load(x, cb16 < 8 ? wr : wi, ws, w - 4, lane, rb, cb16, b, tag);
    PRIO(3);
    const Flag fa(threadIdx.x == 0, FA + hid, tag);
    WAITVL_BAR();
    fa.raise(rs);
    u32x4 wl[10];
    if (PROBE == 4) HOLD(t0, 33);
    if (w < 4) {
        if (hid < 200) retile_wo(wo, rs, hid * 4 + w, lane);
        seq_compute(rs, w, lane, kq, mt, nt, hid, tag);
#pragma unroll
        for (int i = 0; i < 10; ++i) wl[i] = lds128(69632 + lane * 16 + i * 1024);
    } else seq_load(kq < 2 ? cr : ci, ws, w - 4, lane, kq, mt, nt, cb, lane < 25 ? FW + 25 * cb + lane : FQ + (rbk >> 1) * 16 + 15, tag, rs, wl);
    const unsigned ts = w & 3, kh = w >> 2, g = lane >> 4, tt = rbk * 2 + (ts >> 1), tq = (ts & 1) * 16 + (lane & 15), l16 = lane * 16;
    const float gate = __uint_as_float(ld32(rs, tq * 4, WS_G + tt * 128));
    const Flag fp(threadIdx.x == 0, FP + (mt * 4 + nt) * 4 + kq, tag);
    WAITVL_BAR();
    fp.raise(rs);
    if (w >= 6) { const unsigned a = 69632 + (w - 6) * 5120; dma5c(l16, ws + WS_WT + cb * 102400 + a, a); }
    u32x4 pa[4]; u32x2 pb[4];
    pa[0] = lds128(59392 + (2 * ts + kh) * 1024 + l16); pb[0] = lds64(59392 + (8 + (ts >> 1)) * 1024 + (ts & 1) * 8 + l16);
    if (w == 4) await<NA>(ws, FP + (mt * 4 + nt) * 4 + (lane >> 4), tag);
    WAITL_BAR();
    if (w >= 6) { const unsigned a = 59392 + (w - 6) * 5120; dma5c(l16, ws + WS_WT + cb * 102400 + a, a); }
    const unsigned ps = WS_P + (rbk * 8 + cb) * 10240;
#pragma unroll
    for (int q = 1; q < 4; ++q) { pa[q] = ld128(rs, l16, ps + (q ^ kq) * (256 * 10240) + (2 * ts + kh) * 1024); pb[q] = ld64(rs, l16, kh ? WS_END : ps + (q ^ kq) * (256 * 10240) + (8 + (ts >> 1)) * 1024 + (ts & 1) * 8); }
    SBAR();
    PRIO(0);
    SBAR();
    float y[12];
#pragma unroll
    for (int e = 0; e < 4; ++e) { y[2 * e] = (bflo(pa[0][e]) + bflo(pa[1][e])) - (bflo(pa[2][e]) + bflo(pa[3][e])); y[2 * e + 1] = (bfhi(pa[0][e]) + bfhi(pa[1][e])) - (bfhi(pa[2][e]) + bfhi(pa[3][e])); }
#pragma unroll
    for (int e = 0; e < 2; ++e) { y[8 + 2 * e] = (bflo(pb[0][e]) + bflo(pb[1][e])) - (bflo(pb[2][e]) + bflo(pb[3][e])); y[9 + 2 * e] = (bfhi(pb[0][e]) + bfhi(pb[1][e])) - (bfhi(pb[2][e]) + bfhi(pb[3][e])); }
#pragma unroll
    for (int i = 0; i < 12; ++i) { y[i] *= kq < 2 ? gate : -gate; PIN(y[i]); }
    float og = 1.f - gate;
    unsigned aw = kh * 40960 + l16, vo = g * 512 + tq * 16, vo2 = g * 512 + tq * 8;
    unsigned oa = (tt * 32 + (ts & 1) * 16 + (lane >> 2)) * 1280 + cb * 160 + kh * 64 + (lane & 3) * 16, ob = kh ? 2048 * 1280 : oa + 128 - (lane & 3) * 8, op = (lane & 3) * 64 + (lane & ~3);
    unsigned o1[4], o2 = WS_O2 + (tt * 32 + 16 * kh) * 256;
#pragma unroll
    for (int i = 0; i < 4; ++i) { o1[i] = WS_O1 + (tt * 32 + 16 * kh) * 1024 + i * 4096; PINS(o1[i]); }
    PIN(og); PIN(oa); PIN(ob); PIN(op); PIN(aw); PIN(vo); PIN(vo2); PINS(o2);
    if (w == 4) await<NO>(ws, FO + (rbk >> 2) * 32 + (lane >> 1), tag);
    WAITV_BAR(0);
    u32x4 of[10];
#pragma unroll
    for (int i = 0; i < 2; ++i) { const u32x2 lo = ld64(rs, vo2 + i * 2048, o2), hi = ld64(rs, vo2 + i * 2048 + 256, o2); of[i] = (u32x4){lo[0], lo[1], hi[0], hi[1]}; }
#pragma unroll
    for (int i = 0; i < 8; ++i) of[2 + i] = ld128(rs, vo + (i & 1) * 2048, o1[i >> 1]);
    SBAR();
    PRIO(3);
    f32x4 acc[5];
#pragma unroll
    for (int n = 0; n < 50; ++n) {
        acc[n % 5] = mma16(wl[n % 10], of[n / 5], n < 5 ? f32x4{} : acc[n % 5]);
        if (n < 40) wl[n % 10] = lds128(aw + n * 1024);
        if (n == 44) BAR();
        SBAR();
    }
    LAS f32x4* mine = (LAS f32x4*)(uintptr_t)(aw + ts * 3072);
    const LAS f32x4* other = (const LAS f32x4*)(uintptr_t)((aw ^ 40960) + ts * 3072);
    mine[0] = acc[2]; mine[64] = acc[3]; mine[128] = acc[4];
    WAITL_BAR();
    const f32x4 a0 = acc[0] + other[0], a1 = acc[1] + other[64], a2 = acc[4] + other[128];
    u32x4 r0 = {pk2(og * a0[0] + y[0], og * a0[1] + y[1]), pk2(og * a0[2] + y[2], og * a0[3] + y[3]), pk2(og * a1[0] + y[4], og * a1[1] + y[5]), pk2(og * a1[2] + y[6], og * a1[3] + y[7])};
    u32x2 r1 = {pk2(og * a2[0] + y[8], og * a2[1] + y[9]), pk2(og * a2[2] + y[10], og * a2[3] + y[11])};
#pragma unroll
    for (int i = 0; i < 4; ++i) r0[i] = __builtin_amdgcn_ds_bpermute(op, r0[i]);
#pragma unroll
    for (int i = 0; i < 2; ++i) r1[i] = __builtin_amdgcn_ds_bpermute(op, r1[i]);
    const rsrc_t ro = __builtin_amdgcn_make_buffer_rsrc(out, 0, 2048 * 1280, 0x00020000);
    __builtin_amdgcn_raw_buffer_store_b128(r0, ro, oa, 0, 0);
    __builtin_amdgcn_raw_buffer_store_b64(r1, ro, ob, 0, 0);
}

__global__ __launch_bounds__(512, 4) void fused_mix(const char* x, const char* cr, const char* ci, const char* wr, const char* wi, const char* wqkv, const char* wo, const char* gw, char* ws, char* out) {
    const int lane = threadIdx.x & 63, w = __builtin_amdgcn_readfirstlane(threadIdx.x >> 6);
    const unsigned t0 = (unsigned)__builtin_amdgcn_s_memrealtime();
    const rsrc_t rs = __builtin_amdgcn_make_buffer_rsrc(ws, 0, WS_END, 0x00020000);
    const int b = blockIdx.x, rb = (b & 1) * 8 + ((b >> 3) & 7), cg = ((b >> 1) & 3) + 4 * (b >> 6);
    if ((b >> 3) == 32 && w < 3) {
        const char* pc; asm volatile("s_getpc_b64 %0" : "=s"(pc));
        const unsigned t = *(const GAS unsigned*)(pc + min((unsigned)w * 8192 + lane * 128, CODE_BYTES));
        asm volatile("" :: "v"(t));
    }
    if (w >= 4 && cg < 15) { PRIO(3); qkv_load(x, wqkv, w - 4, lane, rb, cg); }
    if (b >= 256) { PRIO(2); role_g(x, cr, ci, wr, wi, wo, ws, out, rs, w, lane, b - 256, t0); }
    else { PRIO(3); role_a(x, gw, ws, rs, w, lane, b, rb, cg, t0); }
    if (PROBE == 3) HOLD(t0, 80);
}

extern "C" void kernel_launch(void* const* d_in, const int* in_sizes, int n_in, void* d_out, int out_size, void* d_ws, size_t ws_size, hipStream_t stream) {
    (void)hipFuncSetAttribute((const void*)fused_mix, hipFuncAttributeMaxDynamicSharedMemorySize, LDS_BYTES);
    fused_mix<<<512, 512, LDS_BYTES, stream>>>((const char*)d_in[0], (const char*)d_in[1], (const char*)d_in[2], (const char*)d_in[3], (const char*)d_in[4],
                                               (const char*)d_in[6], (const char*)d_in[7], (const char*)d_in[8], (char*)d_ws, (char*)d_out);
}
```

```cpp
#include <hip/hip_runtime.h>
#include <hip/hip_bf16.h>
#include <stdint.h>

typedef __attribute__((ext_vector_type(8))) __bf16 bf16x8;
typedef __attribute__((ext_vector_type(2))) __bf16 bf16x2;
typedef __attribute__((ext_vector_type(4))) short s16x4;
typedef __attribute__((ext_vector_type(16))) float f32x16;
typedef __attribute__((ext_vector_type(4))) float f32x4;
typedef __attribute__((ext_vector_type(2))) float f32x2;
typedef __attribute__((ext_vector_type(4))) unsigned u32x4;
typedef __attribute__((ext_vector_type(2))) unsigned u32x2;
typedef unsigned long long u64;
typedef __amdgpu_buffer_rsrc_t rsrc_t;
#define LAS __attribute__((address_space(3)))
#define GAS __attribute__((address_space(1)))
#define DI __device__ __forceinline__
#define SBAR() __builtin_amdgcn_sched_barrier(0)
#define PIN(x) asm volatile("" : "+v"(x))
#define PINS(x) asm volatile("" : "+s"(x))
#define PRIO(n) __builtin_amdgcn_s_setprio(n)

constexpr unsigned REC = 3072, KV_HS = 65 * REC, QT = 1536, Q_HS = 64 * QT;
constexpr unsigned WS_KV = 0;
constexpr unsigned WS_Q = WS_KV + 32 * KV_HS;
constexpr unsigned WS_AB = WS_Q + 32 * Q_HS;
constexpr unsigned WS_P = WS_AB + 8 * 64 * 10240;
constexpr unsigned WS_O1 = WS_P + 4 * 256 * 10240;
constexpr unsigned WS_O2 = WS_O1 + 64 * 32 * 1024;
constexpr unsigned WS_WT = WS_O2 + 64 * 32 * 256;
constexpr unsigned WS_G = WS_WT + 800 * 1024;
constexpr unsigned WS_F = WS_G + 8192;
constexpr unsigned WS_E = WS_F + 6 * 256 * 128;
constexpr unsigned WS_END = WS_E + 128;
constexpr unsigned FQ = 0, FA = 256, FP = 512, FW = 768, FL = 1024, FO = 1280;
constexpr unsigned LDS_BYTES = 81920, CODE_BYTES = 24448;
constexpr float QSCALE = 0.22360679774997896f * 1.4426950408889634f;
#define PACE 3
#define AAT 6
#define NL 8
#define NA 8
#define NO 1
#define PRE 0
#define GSW 2

DI unsigned swap23(unsigned r) { return (r & ~12u) | ((r & 4u) << 1) | ((r & 8u) >> 1); }
DI unsigned pk2(float a, float b) { f32x2 v = {a, b}; return __builtin_bit_cast(unsigned, __builtin_convertvector(v, bf16x2)); }
DI float bflo(unsigned u) { return __uint_as_float(u << 16); }
DI float bfhi(unsigned u) { return __uint_as_float(u & 0xffff0000u); }
DI f32x16 mma32(u32x4 a, u32x4 b, f32x16 c) { return __builtin_amdgcn_mfma_f32_32x32x16_bf16(__builtin_bit_cast(bf16x8, a), __builtin_bit_cast(bf16x8, b), c, 0, 0, 0); }
DI f32x16 mma32k8(u32x2 a, u32x2 b, f32x16 c) { return __builtin_amdgcn_mfma_f32_32x32x8bf16_1k(__builtin_bit_cast(s16x4, a), __builtin_bit_cast(s16x4, b), c, 0, 0, 0); }
DI f32x4 mma16(u32x4 a, u32x4 b, f32x4 c) { return __builtin_amdgcn_mfma_f32_16x16x32_bf16(__builtin_bit_cast(bf16x8, a), __builtin_bit_cast(bf16x8, b), c, 0, 0, 0); }
DI u32x4 pk8(const f32x16& a, int s, float c) { return (u32x4){pk2(a[8 * s] * c, a[8 * s + 1] * c), pk2(a[8 * s + 2] * c, a[8 * s + 3] * c), pk2(a[8 * s + 4] * c, a[8 * s + 5] * c), pk2(a[8 * s + 6] * c, a[8 * s + 7] * c)}; }
DI u32x4 pk8(const f32x16& a, int s) { return (u32x4){pk2(a[8 * s], a[8 * s + 1]), pk2(a[8 * s + 2], a[8 * s + 3]), pk2(a[8 * s + 4], a[8 * s + 5]), pk2(a[8 * s + 6], a[8 * s + 7])}; }
DI u32x4 pk44(f32x4 a, f32x4 b) { return (u32x4){pk2(a[0], a[1]), pk2(a[2], a[3]), pk2(b[0], b[1]), pk2(b[2], b[3])}; }
DI f32x16 zero16() { u32x2 z = {0u, 0u}; PIN(z); return mma32k8(z, z, f32x16{}); }
DI u32x4 lds128(unsigned a) { return *(const LAS u32x4*)(uintptr_t)a; }
DI u32x2 lds64(unsigned a) { return *(const LAS u32x2*)(uintptr_t)a; }
DI u32x2 ldstr(unsigned a) { return __builtin_bit_cast(u32x2, __builtin_amdgcn_ds_read_tr16_b64_v4i16((LAS s16x4*)(uintptr_t)a)); }
DI void st128(rsrc_t rs, unsigned v, unsigned s, u32x4 d) { __builtin_amdgcn_raw_buffer_store_b128(d, rs, v, s, 16); asm volatile("s_nop 1" :: "v"(d) : "memory"); }
DI void st64(rsrc_t rs, unsigned v, unsigned s, u32x2 d) { __builtin_amdgcn_raw_buffer_store_b64(d, rs, v, s, 16); }
DI void st32(rsrc_t rs, unsigned v, unsigned s, unsigned d) { __builtin_amdgcn_raw_buffer_store_b32(d, rs, v, s, 16); }
DI u32x4 ld128(rsrc_t rs, unsigned v, unsigned s) { return __builtin_amdgcn_raw_buffer_load_b128(rs, v, s, 16); }
DI u32x2 ld64(rsrc_t rs, unsigned v, unsigned s) { return __builtin_amdgcn_raw_buffer_load_b64(rs, v, s, 16); }
DI unsigned ld32(rsrc_t rs, unsigned v, unsigned s) { return __builtin_amdgcn_raw_buffer_load_b32(rs, v, s, 16); }
#define M0SET "s_mov_b32 m0, %[l]\n\ts_nop 0\n\t"
#define GLD(v, o, c) "global_load_lds_dwordx4 %[" v "], %[b] offset:" o c "\n\t"
DI void dma1(unsigned v0, const char* base, unsigned lds) { asm volatile(M0SET GLD("v0", "0", "") ::[v0] "v"(v0), [b] "s"(base), [l] "s"(lds) : "memory"); }
DI void dma2(unsigned v0, unsigned v1, const char* base, unsigned lds) { asm volatile(M0SET GLD("v0", "0", "") GLD("v1", "1024", "") ::[v0] "v"(v0), [v1] "v"(v1), [b] "s"(base), [l] "s"(lds) : "memory"); }
DI void dma4(unsigned v0, unsigned v1, unsigned v2, unsigned v3, const char* base, unsigned lds) {
    asm volatile(M0SET GLD("v0", "0", "") GLD("v1", "1024", "") GLD("v2", "2048", "") GLD("v3", "3072", "") ::[v0] "v"(v0), [v1] "v"(v1), [v2] "v"(v2), [v3] "v"(v3), [b] "s"(base), [l] "s"(lds) : "memory");
}
DI void dma1c(unsigned v0, const char* base, unsigned lds) { asm volatile(M0SET GLD("v0", "0", " sc1") ::[v0] "v"(v0), [b] "s"(base), [l] "s"(lds) : "memory"); }
DI void dma4c(unsigned v0, const char* base, unsigned lds) {
    asm volatile(M0SET GLD("v0", "0", " sc1") GLD("v0", "1024", " sc1") GLD("v0", "2048", " sc1") GLD("v0", "3072", " sc1") ::[v0] "v"(v0), [b] "s"(base), [l] "s"(lds) : "memory");
}
DI void dma5c(unsigned v0, const char* base, unsigned lds) { dma4c(v0, base, lds); dma1c(v0, base + 4096, lds + 4096); }
DI void dma_rec(unsigned v0, unsigned v1, const char* base, unsigned lds) {
    asm volatile(M0SET GLD("v0", "0", " sc1") "s_mov_b32 exec_hi, 0xff0fffff\n\t" GLD("v1", "1024", " sc1") "s_mov_b32 exec_lo, 0xffff0fff\n\ts_mov_b32 exec_hi, 0x0fffff0f\n\t" GLD("v0", "2048", " sc1") "s_mov_b64 exec, -1"
                 ::[v0] "v"(v0), [v1] "v"(v1), [b] "s"(base), [l] "s"(lds) : "memory");
}
#define BAR() asm volatile("s_barrier" ::: "memory")
#define WAITV_BAR(N) asm volatile("s_waitcnt vmcnt(" #N ")\n\ts_barrier" ::: "memory")
#define WAITL_BAR() asm volatile("s_waitcnt lgkmcnt(0)\n\ts_barrier" ::: "memory")
#define WAITVL_BAR() asm volatile("s_waitcnt vmcnt(0) lgkmcnt(0)\n\ts_barrier" ::: "memory")

DI u64 ldflag(const char* ws, unsigned off) { return __hip_atomic_load((const GAS u64*)(ws + off), __ATOMIC_RELAXED, __HIP_MEMORY_SCOPE_AGENT); }
DI void stflag(char* ws, unsigned off, u64 v) { __hip_atomic_store((GAS u64*)(ws + off), v, __ATOMIC_RELAXED, __HIP_MEMORY_SCOPE_AGENT); }
DI u64 mktag(u64 e) {
    const u64 u = ((u64)(unsigned)__builtin_amdgcn_readfirstlane((unsigned)(e >> 32)) << 32) | (unsigned)__builtin_amdgcn_readfirstlane((unsigned)e);
    return u * 6364136223846793005ull + 1442695040888963407ull;
}
struct Flag {
    unsigned v; u32x2 d;
    DI Flag(bool mine, unsigned f, u64 tag) { v = mine ? WS_F + f * 128 : WS_END; d = (u32x2){(unsigned)tag, (unsigned)(tag >> 32)}; PIN(v); PIN(d); }
    DI void raise(rsrc_t rs) const { st64(rs, v, 0, d); }
};
template <int NAP = 1> DI void await(const char* ws, unsigned f0, u64 tag) {
    u64 a; unsigned n = 1u << 19;
    asm volatile("1:\n\tglobal_load_dwordx2 %0, %2, %3 sc1\n\ts_sub_u32 %1, %1, 1\n\ts_sleep %c5\n\ts_waitcnt vmcnt(0)\n\tv_cmp_ne_u64 vcc, %4, %0\n\ts_cbranch_scc1 2f\n\ts_cbranch_vccnz 1b\n2:"
                 : "=&v"(a), "+s"(n) : "v"(WS_F + f0 * 128), "s"(ws), "s"(tag), "i"(NAP) : "vcc", "scc", "memory");
}
DI void await(const char* ws, unsigned f0, unsigned f1, u64 tag) {
    u64 a, b, m; unsigned n = 1u << 19;
    asm volatile("1:\n\tglobal_load_dwordx2 %0, %4, %6 sc1\n\tglobal_load_dwordx2 %1, %5, %6 sc1\n\ts_sub_u32 %3, %3, 1\n\ts_cbranch_scc1 2f\n\ts_waitcnt vmcnt(0)\n\t"
                 "v_cmp_ne_u64 %2, %7, %0\n\tv_cmp_ne_u64 vcc, %7, %1\n\ts_or_b64 vcc, vcc, %2\n\ts_cbranch_scc1 1b\n2:\n\ts_waitcnt vmcnt(0)"
                 : "=&v"(a), "=&v"(b), "=&s"(m), "+s"(n) : "v"(WS_F + f0 * 128), "v"(WS_F + f1 * 128), "s"(ws), "s"(tag) : "vcc", "scc", "memory");
}

template <int T>
DI void qkv_compute(rsrc_t rs, int w, int lane, int rb, int cg, int b, u64 ep) {
    const unsigned r = lane & 31, hh = lane >> 5, wm = w & 1, wn = w >> 1, q = (lane & 15) >> 2, p = lane & 3;
    const unsigned pp = T == 0 ? ((p & 1) << 1) | (p >> 1) : p;
    unsigned xa = (64 * wm + r) * 128 + ((hh ^ ((r >> 1) & 7)) << 4), wa[2];
    wa[0] = 49152 + (8 * hh + q) * 256 + (((2 * wn) ^ q) << 6) + ((lane >> 4) & 1) * 32 + pp * 8; wa[1] = wa[0] ^ 64;
    PIN(xa); PIN(wa[0]); PIN(wa[1]);
    const unsigned kt = rb * 4 + 2 * wm;
    const bool isk = cg >= 5;
    unsigned va[4], sb, ts = REC;
    if (T == 0) {
        const unsigned ct = isk ? cg - 5 : cg, hs = isk ? KV_HS : Q_HS, slot = isk ? r : swap23(r);
        ts = isk ? REC : QT; sb = (isk ? WS_KV : WS_Q) + kt * ts;
#pragma unroll
        for (int i = 0; i < 4; ++i) { const unsigned oc = ct * 16 + 8 * wn + 2 * i + hh, hp = oc / 5, ty = oc - hp * 5; va[i] = (2 * hp + (ty > 2)) * hs + (ty > 2 ? ty - 3 : ty) * 512 + slot * 16; PIN(va[i]); }
    } else {
        sb = WS_KV + kt * REC + 1536;
#pragma unroll
        for (int i = 0; i < 2; ++i) { const unsigned c = (cg - 10) * 128 + 64 * wn + 32 * i + r, head = c / 20, d = c - head * 20; va[i] = head * KV_HS + hh * 384 + (d < 16 ? swap23(d) : d) * 16; PIN(va[i]); }
    }
    f32x16 acc[2][2];
#pragma unroll
    for (int i = 0; i < 4; ++i) { acc[i >> 1][i & 1] = zero16(); PIN(acc[i >> 1][i & 1]); }
    u32x4 xf[2][2], wf[2][2];
    auto rd_x = [&](int set, int g, int mi) { xf[set][mi] = lds128((xa ^ ((g & 3) << 5)) + mi * 4096); };
    auto rd_w = [&](int set, int g, int ni) {
        const unsigned a = wa[ni] + (g & 7) * 4096;
        const u32x2 lo = ldstr(a), hi = ldstr(a + 1024); wf[set][ni] = (u32x4){lo[0], lo[1], hi[0], hi[1]};
    };
    WAITL_BAR();
    rd_x(0, 0, 0); rd_w(0, 0, 0); rd_x(0, 0, 1); rd_w(0, 0, 1);
    unsigned xs = 0;
#pragma unroll 1
    for (int t = 0; t < 5; ++t) {
        if (PRE && t < 4) {
#pragma unroll
            for (int s = 0; s < 2; ++s) st128(rs, T == 0 ? (t & 2 ? va[2 + s] : va[s]) : (t & 2 ? va[1] : va[0]), T == 0 ? sb + (t & 1) * ts : sb + ((t & 1) + s) * REC + s * 768, xf[0][0]);
        }
        if (__builtin_expect(t == PACE && w == 0, 0)) Flag(lane == 0, FL + b, mktag(ep)).raise(rs);
#pragma unroll
        for (int g = 0; g < 8; ++g) {
            const int c = g & 1, n = c ^ 1;
            if (c) WAITL_BAR();
            if ((g & 3) == 3) { const unsigned d = xs == 32768 ? -32768 : 16384; xs += d; xa += d; }
            SBAR();
#pragma unroll
            for (int i = 0; i < 4; ++i) {
                const int mi = i & 1, ni = i >> 1;
                acc[mi][ni] = T == 0 ? mma32(wf[c][ni], xf[c][mi], acc[mi][ni]) : mma32(xf[c][mi], wf[c][ni], acc[mi][ni]);
                if (i == 0) { rd_x(n, g + 1, 0); rd_w(n, g + 1, 0); }
                if (i == 1) { rd_x(n, g + 1, 1); rd_w(n, g + 1, 1); }
                SBAR();
            }
        }
    }
    const float sc = isk ? 1.f : QSCALE;
#pragma unroll
    for (int i = 0; i < 8; ++i) {
        const int mi = (i >> 1) & 1, ni = i >> 2, s = i & 1;
        if (T == 0) st128(rs, va[2 * ni + s], sb + mi * ts, pk8(acc[mi][ni], s, sc));
        else st128(rs, va[ni], sb + (mi + s) * REC + s * 768, pk8(acc[mi][ni], s));
    }
}

DI void qkv_load(const char* x, const char* wqkv, int li, int lane, int rb, int cg) {
    const int rot = 5 * ((rb ^ (cg >> 2)) & 1);
    if (li >= 2) {
        unsigned v0 = (lane >> 4) * 3840 + ((((lane >> 2) & 3) ^ (lane >> 4)) << 6) + (lane & 3) * 16, v1 = v0 + 14336, v2 = v0 + 2 * 14336, v3 = v0 + 3 * 14336;
        unsigned off = cg * 256 + (16 * (li - 2) + 64 * rot) * 3840, lds = 49152 + (li - 2) * 4096;
        PIN(v0); PIN(v1); PIN(v2); PIN(v3); PINS(off); PINS(lds); SBAR();
        const char* base = wqkv + off;
        unsigned slot = 0; int left = 20 - 2 * rot;
        auto issue = [&]() { dma4(v0, v1, v2, v3, base, lds + slot); base += --left ? 32 * 3840 : (32 - 640) * 3840; slot = (slot + 8192) & 32767; };
        issue(); issue();
        WAITV_BAR(4);
        issue();
#pragma unroll 1
        for (int s = 0; s < 17; ++s) { issue(); WAITV_BAR(8); }
        WAITV_BAR(4); WAITV_BAR(0); BAR();
    } else {
        const unsigned row = lane >> 3;
        unsigned v0 = swap23(row) * 1280 + (((lane & 7) ^ (row >> 1)) << 4), v1 = (v0 ^ 64) + 4 * 1280 - 1024;
        unsigned off = (rb * 128 + 64 * li) * 1280 + rot * 128, lds = li * 8192;
        PIN(v0); PIN(v1); PINS(off); PINS(lds); SBAR();
        const char* base = x + off;
        int left = 10 - rot;
        auto issue = [&]() {
#pragma unroll
            for (int i = 0; i < 4; ++i) dma2(v0, v1, base + i * 16 * 1280, lds + i * 2048);
            base += --left ? 128 : 128 - 1280; lds = lds >= 32768 ? lds - 32768 : lds + 16384;
        };
#pragma unroll 1
        for (int s = 0; s < 2; ++s) issue();
        WAITV_BAR(8);
#pragma unroll 1
        for (int s = 0; s < 8; ++s) { issue(); BAR(); WAITV_BAR(8); }
        BAR(); WAITV_BAR(0); BAR(); BAR();
    }
}

DI void gate_rows(const char* x, const char* gw, rsrc_t rs, int rb) {
    const int row = rb * 128 + (threadIdx.x >> 2), sub = threadIdx.x & 3;
    float s = 0.f;
#pragma unroll 5
    for (int i = 0; i < 20; ++i) {
        const u32x4 a = *(const u32x4*)(x + row * 1280 + (4 * i + sub) * 16), g = *(const u32x4*)(gw + (4 * i + sub) * 16);
#pragma unroll
        for (int e = 0; e < 4; ++e) s += bflo(a[e]) * bflo(g[e]) + bfhi(a[e]) * bfhi(g[e]);
    }
    s += __shfl_xor(s, 1); s += __shfl_xor(s, 2);
    if (sub == 0) st32(rs, row * 4, WS_G, __float_as_uint(1.f / (1.f + __expf(-s))));
}

DI float rowmax16(const f32x16& s) {
    float m = fmaxf(fmaxf(s[0], s[1]), s[2]);
#pragma unroll
    for (int i = 3; i < 15; i += 2) m = fmaxf(fmaxf(m, s[i]), s[i + 1]);
    return fmaxf(m, s[15]);
}
DI float halfmax(float m) { const auto t = __builtin_amdgcn_permlane32_swap(__float_as_uint(m), __float_as_uint(m), false, false); return fmaxf(__uint_as_float(t[0]), __uint_as_float(t[1])); }
DI float upper(float m) { const auto t = __builtin_amdgcn_permlane32_swap(__float_as_uint(m), __float_as_uint(m), false, false); return __uint_as_float(t[1]); }

DI void attention(char* ws, rsrc_t rs, int w, int lane, int head, int qb, u64 tag) {
    const unsigned r = lane & 31, hh = lane >> 5, kh = w >> 2, qt = qb * 8 + 2 * (w & 3), odd = head & 1;
    const unsigned kv = WS_KV + head * KV_HS, kv2 = kv - odd * KV_HS + 1024;
    unsigned l16 = lane * 16, l8 = hh ? WS_END : r * 16 + odd * 8, d0 = l16 + KV_HS, d1 = hh ? d0 : d0 - odd * KV_HS;
    unsigned vg = r < 20 ? 1536 + hh * 384 + r * 16 : WS_END, vfill = r == 20 ? 0x3f803f80u : 0u, kone = hh ? 0x3f80u : 0u;
    const char* src = ws + kv + (32 * kh + 1 + (w & 3)) * REC - KV_HS;
    unsigned dst = w * REC, kvh = kv + 32 * kh * REC, kv2h = kv2 + 32 * kh * REC;
    unsigned qa = WS_Q + head * Q_HS + (qt + 1 - kh) * QT, qa2 = qa + 1024 - odd * Q_HS;
    unsigned o1 = WS_O1 + ((qt + kh) * 32 + head) * 1024, o2 = WS_O2 + ((qt + kh) * 32 + head) * 256;
    const unsigned lb = kh * 4 * REC;
    unsigned ak1 = lb + l16, ak2 = lb + (hh ? 1536 + 22 * 16 : 1024 + r * 16 + odd * 8), av = lb + 1536 + (hh ^ (r > 27)) * 384 + min(r, 21u) * 16;
    unsigned xm = 57344 + w * 3072 + l16, xo = 57344 + (w ^ 4) * 3072 + l16;
    u32x4 p0, p1 = u32x4{}, v1 = u32x4{};
    f32x16 oa = zero16(), ob = zero16();
    float e[16]; e[12] = 0.f; e[13] = 0.f; e[14] = 0.f; e[15] = 0.f;
    PIN(l16); PIN(l8); PIN(d0); PIN(d1); PIN(vg); PIN(vfill); PIN(kone); PIN(ak1); PIN(ak2); PIN(av); PIN(xm); PIN(xo);
    PIN(p1); PIN(v1); PIN(oa); PIN(ob); PIN(e[12]); PIN(e[13]); PIN(e[14]); PIN(e[15]);
    PINS(src); PINS(dst); PINS(kvh); PINS(kv2h); PINS(qa); PINS(qa2); PINS(o1); PINS(o2);
    if (w == 4) {
        const unsigned c0 = (20 * head) >> 7, c1 = (20 * head + 19) >> 7;
        await(ws, FQ + ((lane >> 1) & 15) * 16 + 5 + 5 * (lane >> 5) + ((lane & 1) ? c1 : c0), FQ + (2 * qb + (lane >> 5)) * 16 + ((lane & 16) ? c1 : c0), tag);
    }
    BAR();
    u32x2 k2 = ld64(rs, l8, kv2h), q2a = ld64(rs, l8, qa2);
    u32x4 k1 = ld128(rs, l16, kvh);
    const u32x4 q1a = ld128(rs, l16, qa);
    SBAR();
    u32x4 v0 = ld128(rs, vg, kvh);
    dma_rec(d0, d1, src, dst);
    SBAR();
    f32x16 sa = mma32(k1, q1a, mma32k8(k2, q2a, f32x16{})), sb;
    *(LAS u32x4*)(uintptr_t)(xm + 1024) = q1a;
    unsigned na = pk2(-halfmax(rowmax16(sa)), 0.f); PIN(na);
    q2a[0] = hh ? na : q2a[0]; k2[0] |= kone;
    *(LAS u32x2*)(uintptr_t)xm = q2a;
    sa = mma32(k1, q1a, mma32k8(k2, q2a, f32x16{}));
    SBAR();
    WAITL_BAR();
    u32x2 q2b = lds64(xo);
    const u32x4 q1b = lds128(xo + 1024);
    v0 |= vfill;
    const int jl = kh ? 7 : 0;
#define EX(C, i) do { e[i] = __builtin_amdgcn_exp2f(C[i]); PIN(e[i]); SBAR(); } while (0)
#define CV(P, n, i) do { unsigned t_ = pk2(e[i], e[i + 1]); PIN(t_); P[n] = t_; SBAR(); } while (0)
#define UNIT(C, N, O, OP, Q1, Q2, u)                                                                          \
    do {                                                                                                      \
        SBAR();                                                                                               \
        N = mma32k8(k2, Q2, f32x16{}); SBAR();                                                                \
        if (u == AAT) PRIO(0);                                                                                \
        if (u == 0) {                                                                                         \
            WAITVL_BAR(); PRIO(2);                                                                            \
            if (__builtin_expect(j + jj < jl, 0)) { src += 4 * REC; dma_rec(d0, d1, src - 32 * REC, dst - 4 * REC + 32768 - fl); dma_rec(d0, d1, src, dst + 32768 - fl); } \
        }                                                                                                     \
        if (!(u & 1)) { k2 = lds64(ak2 + (u >> 1) * REC + fl); SBAR(); }                                      \
        EX(C, 0); CV(p1, 2, 12); EX(C, 1); CV(p1, 3, 14); EX(C, 2); EX(C, 3);                                 \
        OP = mma32(v1, p1, OP); SBAR();                                                                       \
        if (!(u & 1)) { v1 = lds128(av + 768 + (u >> 1) * REC + fl); SBAR(); }                                \
        EX(C, 4); CV(p0, 0, 0); EX(C, 5); CV(p0, 1, 2); EX(C, 6); EX(C, 7);                                   \
        N = mma32(k1, Q1, N); SBAR();                                                                         \
        if (!(u & 1)) { k1 = lds128(ak1 + (u >> 1) * REC + fl); SBAR(); }                                     \
        EX(C, 8); CV(p0, 2, 4); EX(C, 9); CV(p0, 3, 6); EX(C, 10); EX(C, 11);                                 \
        O = mma32(v0, p0, O); SBAR();                                                                         \
        if (u & 1) { v0 = lds128(av + (u >> 1) * REC + fl); SBAR(); }                                         \
        EX(C, 12); CV(p1, 0, 8); EX(C, 13); CV(p1, 1, 10); EX(C, 14); EX(C, 15);                              \
    } while (0)
#pragma unroll 1
    for (int j = 0; j < 8; j += 2) {
#pragma unroll
        for (int jj = 0; jj < 2; ++jj) {
            const unsigned fl = jj * 32768;
            UNIT(sa, sb, oa, ob, q1b, q2b, 0); UNIT(sb, sa, ob, oa, q1a, q2a, 1); UNIT(sa, sb, oa, ob, q1b, q2b, 2); UNIT(sb, sa, ob, oa, q1a, q2a, 3);
            UNIT(sa, sb, oa, ob, q1b, q2b, 4); UNIT(sb, sa, ob, oa, q1a, q2a, 5); UNIT(sa, sb, oa, ob, q1b, q2b, 6); UNIT(sb, sa, ob, oa, q1a, q2a, 7);
        }
    }
#undef UNIT
#undef EX
#undef CV
    LAS f32x4* mine = (LAS f32x4*)(uintptr_t)xm;
    const LAS f32x4* other = (const LAS f32x4*)(uintptr_t)xo;
    p1[2] = pk2(e[12], e[13]); p1[3] = pk2(e[14], e[15]);
    ob = mma32(v1, p1, ob);
#pragma unroll
    for (int i = 0; i < 3; ++i) mine[64 * i] = (f32x4){oa[4 * i], oa[4 * i + 1], oa[4 * i + 2], oa[4 * i + 3]};
    WAITL_BAR();
    PRIO(3);
    f32x16 o;
#pragma unroll
    for (int i = 0; i < 3; ++i) {
        const f32x4 t = other[64 * i];
#pragma unroll
        for (int c = 0; c < 4; ++c) o[4 * i + c] = ob[4 * i + c] + t[c];
    }
    float l = upper(o[8]);
    const float mag = ((fabsf(o[0]) + fabsf(o[1])) + (fabsf(o[2]) + fabsf(o[3]))) + ((fabsf(o[4]) + fabsf(o[5])) + (fabsf(o[6]) + fabsf(o[7]))) + ((fabsf(o[8]) + fabsf(o[9])) + (fabsf(o[10]) + fabsf(o[11])));
    if (__builtin_expect(__any(!(mag * 0x1p-20f + l < 0x1p100f) || !(l > 0x1p-40f)), 0)) {
        if (hh) q2b[0] = 0u;
        float m = -INFINITY;
#pragma unroll 1
        for (int t = 0; t < 64; ++t) m = fmaxf(m, rowmax16(mma32(ld128(rs, l16, kv + t * REC), q1b, mma32k8(ld64(rs, l8, kv2 + t * REC), q2b, f32x16{}))));
        m = halfmax(m);
        o = f32x16{};
#pragma unroll 1
        for (int t = 0; t < 64; ++t) {
            const u32x4 va = ld128(rs, vg, kv + t * REC) | vfill, vb = ld128(rs, vg, kv + (t + 1) * REC + 768) | vfill;
            f32x16 s = mma32(ld128(rs, l16, kv + t * REC), q1b, mma32k8(ld64(rs, l8, kv2 + t * REC), q2b, f32x16{}));
#pragma unroll
            for (int i = 0; i < 16; ++i) s[i] = __builtin_amdgcn_exp2f(s[i] - m);
            o = mma32(vb, pk8(s, 1), mma32(va, pk8(s, 0), o));
        }
        l = upper(o[8]);
    }
    const float sc = __builtin_amdgcn_rcpf(l);
    st128(rs, l16, o1, pk8(o, 0, sc));
    st64(rs, hh ? WS_END : l16 >> 1, o2, (u32x2){pk2(o[8] * sc, o[9] * sc), pk2(o[10] * sc, o[11] * sc)});
}

DI void role_a(const char* x, const char* gw, char* ws, rsrc_t rs, int w, int lane, int b, int rb, int cg) {
    const int head = (b & 7) * 4 + ((b >> 3) & 3), qb = b >> 5;
    const u64 ep = ldflag(ws, WS_E);
    if (__builtin_expect(cg == 15, 0)) { gate_rows(x, gw, rs, rb); Flag(threadIdx.x == 0, FL + b, mktag(ep)).raise(rs); }
    if (w >= 4) {
        const unsigned i = threadIdx.x - 256, rec = i >> 4, c = (i & 3) == 0 ? 0x3f803f80u : 0u;
        *(LAS u32x4*)(uintptr_t)((rec >> 3) * 32768 + (rec & 7) * REC + 1536 + ((i >> 2) & 3) * 384 + 320 + (i & 3) * 16) = (u32x4){(i & 3) == 2 ? 0x3f80u : c, c, c, c};
        const Flag fq(threadIdx.x == 320, FQ + rb * 16 + cg, mktag(ep));
        WAITVL_BAR();
        fq.raise(rs);
    } else {
        if (cg < 10) qkv_compute<0>(rs, w, lane, rb, cg, b, ep); else if (cg < 15) qkv_compute<1>(rs, w, lane, rb, cg, b, ep);
        WAITV_BAR(0);
    }
    const u64 tag = mktag(ep);
    attention(ws, rs, w, lane, head, qb, tag);
    const Flag fo(threadIdx.x == 0, FO + qb * 32 + head, tag);
    WAITV_BAR(0);
    fo.raise(rs);
    if (b == 0) {
        await(ws, FQ + threadIdx.x, tag);
        BAR();
        if (threadIdx.x == 0) stflag(ws, WS_E, tag);
    }
}

DI void hid_compute(rsrc_t rs, int w, int lane, int rb, int cb16) {
    const unsigned rr = lane & 15, g = lane >> 4, rel = rr * 128 + ((g ^ (rr >> 1)) << 4);
    unsigned xa = w * 4096 + rel, wa = 49152 + rel;
    f32x4 acc[2][5];
#pragma unroll
    for (int i = 0; i < 10; ++i) { acc[i / 5][i % 5] = f32x4{}; PIN(acc[i / 5][i % 5]); }
    PIN(xa); PIN(wa);
    u32x4 f[2][7];
    auto rd = [&](int set, int ks, int i) { f[set][i] = i < 2 ? lds128((xa ^ (ks << 6)) + i * 2048) : lds128((wa ^ (ks << 6)) + (i - 2) * 2048); };
    WAITL_BAR();
    PRIO(3);
#pragma unroll
    for (int i = 0; i < 7; ++i) rd(0, 0, i);
    unsigned sb = 0;
#pragma unroll 2
    for (int s = 0; s < 10; ++s) {
#pragma unroll
        for (int ks = 0; ks < 2; ++ks) {
            if (ks) { WAITL_BAR(); const int d = sb == 2 ? -2 : 1; sb += d; xa += d * 16384; wa += d * 10240; }
            SBAR();
#pragma unroll
            for (int i = 0; i < 10; ++i) { acc[i & 1][i >> 1] = mma16(f[ks][i & 1], f[ks][2 + (i >> 1)], acc[i & 1][i >> 1]); if (i < 7) rd(ks ^ 1, ks ^ 1, i); SBAR(); }
        }
    }
    const unsigned so = WS_AB + ((cb16 >> 1) * 64 + rb * 4 + w) * 10240 + (cb16 & 1) * 5120;
#pragma unroll
    for (int j = 0; j < 5; ++j) st128(rs, lane * 16 + (j & 3) * 1024, so + (j >> 2) * 4096, pk44(acc[0][j], acc[1][j]));
}

DI void hid_load(const char* x, const char* wd, const char* ws, int li, int lane, int rb, int cb16, int b, u64 tag) {
    const int rot = ((rb & 7) * 5) >> 2;
    const unsigned row = lane >> 3, ev = swap23(row) * 1280 + (((lane & 7) ^ (row >> 1)) << 4);
    int left = 10 - rot;
    if (li < 2) {
        const unsigned v1 = (ev ^ 64) + 16 * 1280 - 1024;
        const char* base = x + (rb * 128 + 64 * li) * 1280 + rot * 128;
        unsigned lds = li * 8192;
        auto issue = [&]() {
#pragma unroll
            for (int i = 0; i < 4; ++i) dma2(ev, v1, base + ((i >> 1) * 32 + (i & 1) * 4) * 1280, lds + i * 2048);
            base += --left ? 128 : 128 - 1280; lds = lds >= 32768 ? lds - 32768 : lds + 16384;
        };
        await<NL>(ws, FL + b, tag);
#pragma unroll 1
        for (int s = 0; s < 3; ++s) { issue(); if (s == 1) WAITV_BAR(8); }
#pragma unroll 1
        for (int s = 0; s < 7; ++s) { WAITV_BAR(8); issue(); }
        WAITV_BAR(8); WAITV_BAR(0); BAR();
    } else {
        const unsigned o = li - 2;
        unsigned vw[5];
#pragma unroll
        for (int i = 0; i < 5; ++i) { const unsigned p = 5 * o + i; vw[i] = (p < 8 ? (ev ^ ((p & 1) << 6)) + (32 * (p >> 2) + 4 * ((p >> 1) & 1) + 16 * (p & 1)) * 1280 : ((ev & 127) ^ ((p & 1) << 6)) + (8 * p + row) * 1280) + 3072 - (i & 3) * 1024; }
        const char* base = wd + (cb16 & 7) * 80 * 1280 + rot * 128 - 3072;
        unsigned lds = 49152 + o * 5120;
        auto issue = [&]() { dma4(vw[0], vw[1], vw[2], vw[3], base, lds); dma1(vw[4], base, lds + 4096); base += --left ? 128 : 128 - 1280; lds = lds >= 49152 + 20480 ? lds - 20480 : lds + 10240; };
        __builtin_amdgcn_s_sleep(32);
#pragma unroll 1
        for (int s = 0; s < 3; ++s) issue();
        WAITV_BAR(10);
#pragma unroll 1
        for (int s = 0; s < 7; ++s) { WAITV_BAR(5); issue(); }
        WAITV_BAR(5); WAITV_BAR(0); BAR();
    }
}

DI void retile_wo(const char* wo, rsrc_t rs, int f, int lane) {
    const int cbk = f / 100, st = (f - cbk * 100) / 5, tile = f % 5, kh = st < 16 ? st >> 3 : (st >> 1) & 1, i = st < 16 ? st & 7 : 8 + (st & 1);
    const int rr = lane & 15, g = lane >> 4;
    const int e = cbk * 80 + (tile < 4 ? 32 * ((tile >> 1) ^ kh) + 8 * (rr >> 2) + 4 * (tile & 1) + (rr & 3) : 64 + rr);
    const int k0 = i < 8 ? (16 * kh + 2 * i + (g >> 1)) * 20 + 8 * (g & 1) : (16 * kh + 8 * (i - 8) + 2 * g) * 20 + 16, k4 = i < 8 ? k0 + 4 : k0 + 20;
    const unsigned short* p = (const unsigned short*)wo + e;
    u32x4 v;
    v[0] = p[k0 * 640] | ((unsigned)p[(k0 + 1) * 640] << 16); v[1] = p[(k0 + 2) * 640] | ((unsigned)p[(k0 + 3) * 640] << 16);
    v[2] = p[k4 * 640] | ((unsigned)p[(k4 + 1) * 640] << 16); v[3] = p[(k4 + 2) * 640] | ((unsigned)p[(k4 + 3) * 640] << 16);
    st128(rs, lane * 16, WS_WT + f * 1024, v);
}

DI void seq_compute(rsrc_t rs, int w, int lane, int kq, int mt, int nt, int hid, u64 tag) {
    const unsigned rr = lane & 15, g = lane >> 4;
    unsigned ac[2], ab = 49152 + (w >> 1) * 5120 + lane * 16;
    ac[0] = ((w & 1) * 64 + rr) * 128 + ((g ^ (rr >> 1)) << 4); ac[1] = ac[0] ^ 64;
    PIN(ac[0]); PIN(ac[1]); PIN(ab);
    f32x4 acc[4][5];
#pragma unroll
    for (int j = 0; j < 5; ++j) {
        f32x16 z = zero16(); PIN(z);
#pragma unroll
        for (int i = 0; i < 4; ++i) acc[i][j] = (f32x4){z[4 * i], z[4 * i + 1], z[4 * i + 2], z[4 * i + 3]};
    }
    u32x4 cf[4], af[3];
    WAITVL_BAR();
    if (w == 0) Flag(lane == 0, FW + hid, tag).raise(rs);
    PRIO(0);
#pragma unroll
    for (int i = 0; i < 4; ++i) cf[i] = lds128(ac[0] + i * 2048);
    af[0] = lds128(ab); af[1] = lds128(ab + 1024);
    auto step = [&](int k) {
        const int n = (k + 1) % 6;
#pragma unroll
        for (int j = 0; j < 5; ++j) {
            if (j == 3) WAITL_BAR();
            SBAR();
            af[(k * 5 + j + 2) % 3] = lds128(ab + (j < 3 ? (k % 3) * 10240 + (j + 2) * 1024 : (n % 3) * 10240 + (j - 3) * 1024));
            SBAR();
#pragma unroll
            for (int i0 = 0; i0 < 4; ++i0) {
                const int i = (j & 1) ? 3 - i0 : i0;
                acc[i][j] = mma16(af[(k * 5 + j) % 3], cf[i], acc[i][j]);
                if (j == 4) cf[i] = lds128(ac[n & 1] + (n >> 1) * 16384 + i * 2048);
                SBAR();
            }
        }
    };
#pragma unroll 1
    for (int t = 0; t < 5; ++t) {
        if (__builtin_amdgcn_readfirstlane(t) == GSW) PRIO(1);
#pragma unroll
        for (int k = 0; k < 6; ++k) step(k);
    }
    step(0); step(1);
    PRIO(3);
    const unsigned sb = WS_P + (kq * 256 + (mt * 2 + (w & 1)) * 8 + nt * 2 + (w >> 1)) * 10240;
    auto part = [&](int i) { return i < 8 ? pk44(acc[i >> 1][2 * (i & 1)], acc[i >> 1][2 * (i & 1) + 1]) : pk44(acc[2 * (i & 1)][4], acc[2 * (i & 1) + 1][4]); };
    if (w == kq) {
#pragma unroll
        for (int i = 0; i < 10; ++i) *(LAS u32x4*)(uintptr_t)(59392 + lane * 16 + i * 1024) = part(i);
    } else {
#pragma unroll
        for (int i = 0; i < 10; ++i) st128(rs, lane * 16 + (i & 3) * 1024, sb + (i >> 2) * 4096, part(i));
    }
}

DI void seq_load(const char* c, const char* ws, int li, int lane, int kq, int mt, int nt, int cb, unsigned fw, u64 tag, rsrc_t rs, u32x4 (&wl)[10]) {
    auto frags = [&]() {
#pragma unroll
        for (int i = 0; i < 10; ++i) wl[i] = ld128(rs, lane * 16 + (i & 3) * 1024, WS_WT + (cb * 100 + 90 + (i & ~3)) * 1024);
    };
    const char* wt = ws + WS_WT + cb * 102400;
    const int rot = (nt & 1) * 8;
    if (li < 2) {
        const unsigned row = lane >> 3, v0 = row * 4096 + (((lane & 7) ^ (row >> 1)) << 4), v1 = (v0 ^ 64) + 8 * 4096 - 1024;
        const char* base = c + (size_t)(mt * 128 + 64 * li) * 4096 + (kq & 1) * 2048 + rot * 128;
        unsigned lds = li * 8192; int left = 16 - rot;
        auto next = [&]() { lds = lds >= 32768 ? lds - 32768 : lds + 16384; };
        auto issue = [&]() {
#pragma unroll
            for (int i = 0; i < 4; ++i) dma2(v0, v1, base + i * 65536, lds + i * 2048);
            base += --left ? 128 : 128 - 2048; next();
        };
#pragma unroll 1
        for (int s = 0; s < 3; ++s) { issue(); if (s == 0) WAITV_BAR(0); }
#pragma unroll 1
        for (int s = 0; s < 13; ++s) { BAR(); WAITV_BAR(8); issue(); }
        frags();
#pragma unroll 1
        for (int s = 0; s < 3; ++s) { BAR(); WAITV_BAR(8); dma4c(lane * 16, wt + lds, lds); dma4c(lane * 16, wt + lds + 4096, lds + 4096); next(); }
    } else {
        const char* bb = ws + WS_AB + (((kq >> 1) * 4 + nt) * 128 + (kq & 1) * 64 + 4 * rot + li - 2) * 5120;
        unsigned lds = 49152 + (li - 2) * 5120; int left = 32 - 2 * rot;
        auto next = [&]() { lds = lds >= 49152 + 20480 ? lds - 20480 : lds + 10240; };
        auto issue = [&]() { dma5c(lane * 16, bb, lds); bb += --left ? 10240 : 10240 - 64 * 5120; next(); };
        await<NA>(ws, FA + ((kq & 1) * 8 + (lane >> 3)) * 16 + (kq >> 1) * 8 + 2 * nt + ((lane >> 2) & 1), tag);
#pragma unroll 1
        for (int s = 0; s < 2; ++s) issue();
        WAITV_BAR(5);
        issue();
#pragma unroll 1
        for (int s = 0; s < 20; ++s) { WAITV_BAR(5); issue(); }
        await(ws, fw, tag);
        frags();
        dma1c(lane * 16, wt + 79872 + (li - 2) * 1024, 79872 + (li - 2) * 1024);
#pragma unroll 1
        for (int s = 20; s < 29; ++s) { WAITV_BAR(5); issue(); }
#pragma unroll 1
        for (int s = 0; s < 2; ++s) { WAITV_BAR(5); dma5c(lane * 16, wt + lds + (s ? 0 : 81920 - 69632), lds); next(); }
        WAITV_BAR(5);
    }
}

DI void role_g(const char* x, const char* cr, const char* ci, const char* wr, const char* wi, const char* wo, char* ws, char* out, rsrc_t rs, int w, int lane, int b) {
    u64 tag = mktag(ldflag(ws, WS_E));
    PINS(tag);
    const int xcd = b & 7, idx = b >> 3;
    const int rb = (xcd & 1) * 8 + (idx & 7), cb16 = (xcd >> 1) * 4 + (idx >> 3), hid = rb * 16 + cb16;
    const int kq = xcd >> 1, mt = (xcd & 1) * 8 + (idx >> 2), nt = idx & 3;
    const int rbk = 2 * mt + (kq & 1), cb = 2 * nt + (kq >> 1);
    if (w < 4) hid_compute(rs, w, lane, rb, cb16);
    else hid_load(x, cb16 < 8 ? wr : wi, ws, w - 4, lane, rb, cb16, b, tag);
    PRIO(3);
    const Flag fa(threadIdx.x == 0, FA + hid, tag);
    WAITVL_BAR();
    fa.raise(rs);
    u32x4 wl[10];
    if (w < 4) {
        if (hid < 200) retile_wo(wo, rs, hid * 4 + w, lane);
        seq_compute(rs, w, lane, kq, mt, nt, hid, tag);
#pragma unroll
        for (int i = 0; i < 10; ++i) wl[i] = lds128(69632 + lane * 16 + i * 1024);
    } else seq_load(kq < 2 ? cr : ci, ws, w - 4, lane, kq, mt, nt, cb, lane < 25 ? FW + 25 * cb + lane : FQ + (rbk >> 1) * 16 + 15, tag, rs, wl);
    const unsigned ts = w & 3, kh = w >> 2, g = lane >> 4, tt = rbk * 2 + (ts >> 1), tq = (ts & 1) * 16 + (lane & 15), l16 = lane * 16;
    const float gate = __uint_as_float(ld32(rs, tq * 4, WS_G + tt * 128));
    const Flag fp(threadIdx.x == 0, FP + (mt * 4 + nt) * 4 + kq, tag);
    WAITVL_BAR();
    fp.raise(rs);
    if (w >= 6) { const unsigned a = 69632 + (w - 6) * 5120; dma5c(l16, ws + WS_WT + cb * 102400 + a, a); }
    u32x4 pa[4]; u32x2 pb[4];
    pa[0] = lds128(59392 + (2 * ts + kh) * 1024 + l16); pb[0] = lds64(59392 + (8 + (ts >> 1)) * 1024 + (ts & 1) * 8 + l16);
    if (w == 4) await<NA>(ws, FP + (mt * 4 + nt) * 4 + (lane >> 4), tag);
    WAITL_BAR();
    if (w >= 6) { const unsigned a = 59392 + (w - 6) * 5120; dma5c(l16, ws + WS_WT + cb * 102400 + a, a); }
    const unsigned ps = WS_P + (rbk * 8 + cb) * 10240;
#pragma unroll
    for (int q = 1; q < 4; ++q) { pa[q] = ld128(rs, l16, ps + (q ^ kq) * (256 * 10240) + (2 * ts + kh) * 1024); pb[q] = ld64(rs, l16, kh ? WS_END : ps + (q ^ kq) * (256 * 10240) + (8 + (ts >> 1)) * 1024 + (ts & 1) * 8); }
    SBAR();
    PRIO(0);
    SBAR();
    float y[12];
#pragma unroll
    for (int e = 0; e < 4; ++e) { y[2 * e] = (bflo(pa[0][e]) + bflo(pa[1][e])) - (bflo(pa[2][e]) + bflo(pa[3][e])); y[2 * e + 1] = (bfhi(pa[0][e]) + bfhi(pa[1][e])) - (bfhi(pa[2][e]) + bfhi(pa[3][e])); }
#pragma unroll
    for (int e = 0; e < 2; ++e) { y[8 + 2 * e] = (bflo(pb[0][e]) + bflo(pb[1][e])) - (bflo(pb[2][e]) + bflo(pb[3][e])); y[9 + 2 * e] = (bfhi(pb[0][e]) + bfhi(pb[1][e])) - (bfhi(pb[2][e]) + bfhi(pb[3][e])); }
#pragma unroll
    for (int i = 0; i < 12; ++i) { y[i] *= kq < 2 ? gate : -gate; PIN(y[i]); }
    float og = 1.f - gate;
    unsigned aw = kh * 40960 + l16, vo = g * 512 + tq * 16, vo2 = g * 512 + tq * 8;
    unsigned oa = (tt * 32 + (ts & 1) * 16 + (lane >> 2)) * 1280 + cb * 160 + kh * 64 + (lane & 3) * 16, ob = kh ? 2048 * 1280 : oa + 128 - (lane & 3) * 8, op = (lane & 3) * 64 + (lane & ~3);
    unsigned o1[4], o2 = WS_O2 + (tt * 32 + 16 * kh) * 256;
#pragma unroll
    for (int i = 0; i < 4; ++i) { o1[i] = WS_O1 + (tt * 32 + 16 * kh) * 1024 + i * 4096; PINS(o1[i]); }
    PIN(og); PIN(oa); PIN(ob); PIN(op); PIN(aw); PIN(vo); PIN(vo2); PINS(o2);
    if (w == 4) await<NO>(ws, FO + (rbk >> 2) * 32 + (lane >> 1), tag);
    WAITV_BAR(0);
    u32x4 of[10];
#pragma unroll
    for (int i = 0; i < 2; ++i) { const u32x2 lo = ld64(rs, vo2 + i * 2048, o2), hi = ld64(rs, vo2 + i * 2048 + 256, o2); of[i] = (u32x4){lo[0], lo[1], hi[0], hi[1]}; }
#pragma unroll
    for (int i = 0; i < 8; ++i) of[2 + i] = ld128(rs, vo + (i & 1) * 2048, o1[i >> 1]);
    SBAR();
    PRIO(3);
    f32x4 acc[5];
#pragma unroll
    for (int n = 0; n < 50; ++n) {
        acc[n % 5] = mma16(wl[n % 10], of[n / 5], n < 5 ? f32x4{} : acc[n % 5]);
        if (n < 40) wl[n % 10] = lds128(aw + n * 1024);
        if (n == 44) BAR();
        SBAR();
    }
    LAS f32x4* mine = (LAS f32x4*)(uintptr_t)(aw + ts * 3072);
    const LAS f32x4* other = (const LAS f32x4*)(uintptr_t)((aw ^ 40960) + ts * 3072);
    mine[0] = acc[2]; mine[64] = acc[3]; mine[128] = acc[4];
    WAITL_BAR();
    const f32x4 a0 = acc[0] + other[0], a1 = acc[1] + other[64], a2 = acc[4] + other[128];
    u32x4 r0 = {pk2(og * a0[0] + y[0], og * a0[1] + y[1]), pk2(og * a0[2] + y[2], og * a0[3] + y[3]), pk2(og * a1[0] + y[4], og * a1[1] + y[5]), pk2(og * a1[2] + y[6], og * a1[3] + y[7])};
    u32x2 r1 = {pk2(og * a2[0] + y[8], og * a2[1] + y[9]), pk2(og * a2[2] + y[10], og * a2[3] + y[11])};
#pragma unroll
    for (int i = 0; i < 4; ++i) r0[i] = __builtin_amdgcn_ds_bpermute(op, r0[i]);
#pragma unroll
    for (int i = 0; i < 2; ++i) r1[i] = __builtin_amdgcn_ds_bpermute(op, r1[i]);
    const rsrc_t ro = __builtin_amdgcn_make_buffer_rsrc(out, 0, 2048 * 1280, 0x00020000);
    __builtin_amdgcn_raw_buffer_store_b128(r0, ro, oa, 0, 0);
    __builtin_amdgcn_raw_buffer_store_b64(r1, ro, ob, 0, 0);
}

__global__ __launch_bounds__(512, 4) void fused_mix(const char* x, const char* cr, const char* ci, const char* wr, const char* wi, const char* wqkv, const char* wo, const char* gw, char* ws, char* out) {
    const int lane = threadIdx.x & 63, w = __builtin_amdgcn_readfirstlane(threadIdx.x >> 6);
    const rsrc_t rs = __builtin_amdgcn_make_buffer_rsrc(ws, 0, WS_END, 0x00020000);
    const int b = blockIdx.x, rb = (b & 1) * 8 + ((b >> 3) & 7), cg = ((b >> 1) & 3) + 4 * (b >> 6);
    if ((b >> 3) == 32 && w < 3) {
        const char* pc; asm volatile("s_getpc_b64 %0" : "=s"(pc));
        const unsigned t = *(const GAS unsigned*)(pc + min((unsigned)w * 8192 + lane * 128, CODE_BYTES));
        asm volatile("" :: "v"(t));
    }
    if (w >= 4 && cg < 15) { PRIO(3); qkv_load(x, wqkv, w - 4, lane, rb, cg); }
    if (b >= 256) { PRIO(2); role_g(x, cr, ci, wr, wi, wo, ws, out, rs, w, lane, b - 256); }
    else { PRIO(3); role_a(x, gw, ws, rs, w, lane, b, rb, cg); }
}

extern "C" void kernel_launch(void* const* d_in, const int* in_sizes, int n_in, void* d_out, int out_size, void* d_ws, size_t ws_size, hipStream_t stream) {
    (void)hipFuncSetAttribute((const void*)fused_mix, hipFuncAttributeMaxDynamicSharedMemorySize, LDS_BYTES);
    fused_mix<<<512, 512, LDS_BYTES, stream>>>((const char*)d_in[0], (const char*)d_in[1], (const char*)d_in[2], (const char*)d_in[3], (const char*)d_in[4],
                                               (const char*)d_in[6], (const char*)d_in[7], (const char*)d_in[8], (char*)d_ws, (char*)d_out);
}
```

```cpp
#include <hip/hip_runtime.h>
#include <hip/hip_bf16.h>
#include <stdint.h>

typedef __attribute__((ext_vector_type(8))) __bf16 bf16x8;
typedef __attribute__((ext_vector_type(2))) __bf16 bf16x2;
typedef __attribute__((ext_vector_type(4))) short s16x4;
typedef __attribute__((ext_vector_type(16))) float f32x16;
typedef __attribute__((ext_vector_type(4))) float f32x4;
typedef __attribute__((ext_vector_type(2))) float f32x2;
typedef __attribute__((ext_vector_type(4))) unsigned u32x4;
typedef __attribute__((ext_vector_type(2))) unsigned u32x2;
typedef unsigned long long u64;
typedef __amdgpu_buffer_rsrc_t rsrc_t;
#define LAS __attribute__((address_space(3)))
#define GAS __attribute__((address_space(1)))
#define DI __device__ __forceinline__
#define SBAR() __builtin_amdgcn_sched_barrier(0)
#define PIN(x) asm volatile("" : "+v"(x))
#define PINS(x) asm volatile("" : "+s"(x))
#define PRIO(n) __builtin_amdgcn_s_setprio(n)

constexpr unsigned REC = 3072, KV_HS = 65 * REC, QT = 1536, Q_HS = 64 * QT;
constexpr unsigned WS_KV = 0;
constexpr unsigned WS_Q = WS_KV + 32 * KV_HS;
constexpr unsigned WS_AB = WS_Q + 32 * Q_HS;
constexpr unsigned WS_P = WS_AB + 8 * 64 * 10240;
constexpr unsigned WS_O1 = WS_P + 4 * 256 * 10240;
constexpr unsigned WS_O2 = WS_O1 + 64 * 32 * 1024;
constexpr unsigned WS_WT = WS_O2 + 64 * 32 * 256;
constexpr unsigned WS_G = WS_WT + 800 * 1024;
constexpr unsigned WS_F = WS_G + 8192;
constexpr unsigned WS_E = WS_F + 6 * 256 * 128;
constexpr unsigned WS_END = WS_E + 128;
constexpr unsigned FQ = 0, FA = 256, FP = 512, FW = 768, FL = 1024, FO = 1280;
constexpr unsigned LDS_BYTES = 81920, CODE_BYTES = 24448;
constexpr float QSCALE = 0.22360679774997896f * 1.4426950408889634f;
#define PACE 3
#define AAT 6
#define NL 8
#define NA 8
#define NO 1
#define PRE 0
#define GSW 2

DI unsigned swap23(unsigned r) { return (r & ~12u) | ((r & 4u) << 1) | ((r & 8u) >> 1); }
DI unsigned pk2(float a, float b) { f32x2 v = {a, b}; return __builtin_bit_cast(unsigned, __builtin_convertvector(v, bf16x2)); }
DI float bflo(unsigned u) { return __uint_as_float(u << 16); }
DI float bfhi(unsigned u) { return __uint_as_float(u & 0xffff0000u); }
DI f32x16 mma32(u32x4 a, u32x4 b, f32x16 c) { return __builtin_amdgcn_mfma_f32_32x32x16_bf16(__builtin_bit_cast(bf16x8, a), __builtin_bit_cast(bf16x8, b), c, 0, 0, 0); }
DI f32x16 mma32k8(u32x2 a, u32x2 b, f32x16 c) { return __builtin_amdgcn_mfma_f32_32x32x8bf16_1k(__builtin_bit_cast(s16x4, a), __builtin_bit_cast(s16x4, b), c, 0, 0, 0); }
DI f32x4 mma16(u32x4 a, u32x4 b, f32x4 c) { return __builtin_amdgcn_mfma_f32_16x16x32_bf16(__builtin_bit_cast(bf16x8, a), __builtin_bit_cast(bf16x8, b), c, 0, 0, 0); }
DI u32x4 pk8(const f32x16& a, int s, float c) { return (u32x4){pk2(a[8 * s] * c, a[8 * s + 1] * c), pk2(a[8 * s + 2] * c, a[8 * s + 3] * c), pk2(a[8 * s + 4] * c, a[8 * s + 5] * c), pk2(a[8 * s + 6] * c, a[8 * s + 7] * c)}; }
DI u32x4 pk8(const f32x16& a, int s) { return (u32x4){pk2(a[8 * s], a[8 * s + 1]), pk2(a[8 * s + 2], a[8 * s + 3]), pk2(a[8 * s + 4], a[8 * s + 5]), pk2(a[8 * s + 6], a[8 * s + 7])}; }
DI u32x4 pk44(f32x4 a, f32x4 b) { return (u32x4){pk2(a[0], a[1]), pk2(a[2], a[3]), pk2(b[0], b[1]), pk2(b[2], b[3])}; }
DI f32x16 zero16() { u32x2 z = {0u, 0u}; PIN(z); return mma32k8(z, z, f32x16{}); }
DI u32x4 lds128(unsigned a) { return *(const LAS u32x4*)(uintptr_t)a; }
DI u32x2 lds64(unsigned a) { return *(const LAS u32x2*)(uintptr_t)a; }
DI u32x2 ldstr(unsigned a) { return __builtin_bit_cast(u32x2, __builtin_amdgcn_ds_read_tr16_b64_v4i16((LAS s16x4*)(uintptr_t)a)); }
DI void st128(rsrc_t rs, unsigned v, unsigned s, u32x4 d) { __builtin_amdgcn_raw_buffer_store_b128(d, rs, v, s, 16); asm volatile("s_nop 1" :: "v"(d) : "memory"); }
DI void st64(rsrc_t rs, unsigned v, unsigned s, u32x2 d) { __builtin_amdgcn_raw_buffer_store_b64(d, rs, v, s, 16); }
DI void st32(rsrc_t rs, unsigned v, unsigned s, unsigned d) { __builtin_amdgcn_raw_buffer_store_b32(d, rs, v, s, 16); }
DI u32x4 ld128(rsrc_t rs, unsigned v, unsigned s) { return __builtin_amdgcn_raw_buffer_load_b128(rs, v, s, 16); }
DI u32x2 ld64(rsrc_t rs, unsigned v, unsigned s) { return __builtin_amdgcn_raw_buffer_load_b64(rs, v, s, 16); }
DI unsigned ld32(rsrc_t rs, unsigned v, unsigned s) { return __builtin_amdgcn_raw_buffer_load_b32(rs, v, s, 16); }
#define M0SET "s_mov_b32 m0, %[l]\n\ts_nop 0\n\t"
#define GLD(v, o, c) "global_load_lds_dwordx4 %[" v "], %[b] offset:" o c "\n\t"
DI void dma1(unsigned v0, const char* base, unsigned lds) { asm volatile(M0SET GLD("v0", "0", "") ::[v0] "v"(v0), [b] "s"(base), [l] "s"(lds) : "memory"); }
DI void dma2(unsigned v0, unsigned v1, const char* base, unsigned lds) { asm volatile(M0SET GLD("v0", "0", "") GLD("v1", "1024", "") ::[v0] "v"(v0), [v1] "v"(v1), [b] "s"(base), [l] "s"(lds) : "memory"); }
DI void dma4(unsigned v0, unsigned v1, unsigned v2, unsigned v3, const char* base, unsigned lds) {
    asm volatile(M0SET GLD("v0", "0", "") GLD("v1", "1024", "") GLD("v2", "2048", "") GLD("v3", "3072", "") ::[v0] "v"(v0), [v1] "v"(v1), [v2] "v"(v2), [v3] "v"(v3), [b] "s"(base), [l] "s"(lds) : "memory");
}
DI void dma1c(unsigned v0, const char* base, unsigned lds) { asm volatile(M0SET GLD("v0", "0", " sc1") ::[v0] "v"(v0), [b] "s"(base), [l] "s"(lds) : "memory"); }
DI void dma4c(unsigned v0, const char* base, unsigned lds) {
    asm volatile(M0SET GLD("v0", "0", " sc1") GLD("v0", "1024", " sc1") GLD("v0", "2048", " sc1") GLD("v0", "3072", " sc1") ::[v0] "v"(v0), [b] "s"(base), [l] "s"(lds) : "memory");
}
DI void dma5c(unsigned v0, const char* base, unsigned lds) { dma4c(v0, base, lds); dma1c(v0, base + 4096, lds + 4096); }
DI void dma_rec(unsigned v0, unsigned v1, const char* base, unsigned lds) {
    asm volatile(M0SET GLD("v0", "0", " sc1") "s_mov_b32 exec_hi, 0xff0fffff\n\t" GLD("v1", "1024", " sc1") "s_mov_b32 exec_lo, 0xffff0fff\n\ts_mov_b32 exec_hi, 0x0fffff0f\n\t" GLD("v0", "2048", " sc1") "s_mov_b64 exec, -1"
                 ::[v0] "v"(v0), [v1] "v"(v1), [b] "s"(base), [l] "s"(lds) : "memory");
}
#define BAR() asm volatile("s_barrier" ::: "memory")
#define WAITV_BAR(N) asm volatile("s_waitcnt vmcnt(" #N ")\n\ts_barrier" ::: "memory")
#define WAITL_BAR() asm volatile("s_waitcnt lgkmcnt(0)\n\ts_barrier" ::: "memory")
#define WAITVL_BAR() asm volatile("s_waitcnt vmcnt(0) lgkmcnt(0)\n\ts_barrier" ::: "memory")

DI u64 ldflag(const char* ws, unsigned off) { return __hip_atomic_load((const GAS u64*)(ws + off), __ATOMIC_RELAXED, __HIP_MEMORY_SCOPE_AGENT); }
DI void stflag(char* ws, unsigned off, u64 v) { __hip_atomic_store((GAS u64*)(ws + off), v, __ATOMIC_RELAXED, __HIP_MEMORY_SCOPE_AGENT); }
DI u64 mktag(u64 e) {
    const u64 u = ((u64)(unsigned)__builtin_amdgcn_readfirstlane((unsigned)(e >> 32)) << 32) | (unsigned)__builtin_amdgcn_readfirstlane((unsigned)e);
    return u * 6364136223846793005ull + 1442695040888963407ull;
}
struct Flag {
    unsigned v; u32x2 d;
    DI Flag(bool mine, unsigned f, u64 tag) { v = mine ? WS_F + f * 128 : WS_END; d = (u32x2){(unsigned)tag, (unsigned)(tag >> 32)}; PIN(v); PIN(d); }
    DI void raise(rsrc_t rs) const { st64(rs, v, 0, d); }
};
template <int NAP = 1> DI void await(const char* ws, unsigned f0, u64 tag) {
    u64 a; unsigned n = 1u << 19;
    asm volatile("1:\n\tglobal_load_dwordx2 %0, %2, %3 sc1\n\ts_sub_u32 %1, %1, 1\n\ts_sleep %c5\n\ts_waitcnt vmcnt(0)\n\tv_cmp_ne_u64 vcc, %4, %0\n\ts_cbranch_scc1 2f\n\ts_cbranch_vccnz 1b\n2:"
                 : "=&v"(a), "+s"(n) : "v"(WS_F + f0 * 128), "s"(ws), "s"(tag), "i"(NAP) : "vcc", "scc", "memory");
}
DI void await(const char* ws, unsigned f0, unsigned f1, u64 tag) {
    u64 a, b, m; unsigned n = 1u << 19;
    asm volatile("1:\n\tglobal_load_dwordx2 %0, %4, %6 sc1\n\tglobal_load_dwordx2 %1, %5, %6 sc1\n\ts_sub_u32 %3, %3, 1\n\ts_cbranch_scc1 2f\n\ts_waitcnt vmcnt(0)\n\t"
                 "v_cmp_ne_u64 %2, %7, %0\n\tv_cmp_ne_u64 vcc, %7, %1\n\ts_or_b64 vcc, vcc, %2\n\ts_cbranch_scc1 1b\n2:\n\ts_waitcnt vmcnt(0)"
                 : "=&v"(a), "=&v"(b), "=&s"(m), "+s"(n) : "v"(WS_F + f0 * 128), "v"(WS_F + f1 * 128), "s"(ws), "s"(tag) : "vcc", "scc", "memory");
}

template <int T>
DI void qkv_compute(rsrc_t rs, int w, int lane, int rb, int cg, int b, u64 ep) {
    const unsigned r = lane & 31, hh = lane >> 5, wm = w & 1, wn = w >> 1, q = (lane & 15) >> 2, p = lane & 3;
    const unsigned pp = T == 0 ? ((p & 1) << 1) | (p >> 1) : p;
    unsigned xa = (64 * wm + r) * 128 + ((hh ^ ((r >> 1) & 7)) << 4), wa[2];
    wa[0] = 49152 + (8 * hh + q) * 256 + (((2 * wn) ^ q) << 6) + ((lane >> 4) & 1) * 32 + pp * 8; wa[1] = wa[0] ^ 64;
    PIN(xa); PIN(wa[0]); PIN(wa[1]);
    const unsigned kt = rb * 4 + 2 * wm;
    const bool isk = cg >= 5;
    unsigned va[4], sb, ts = REC;
    if (T == 0) {
        const unsigned ct = isk ? cg - 5 : cg, hs = isk ? KV_HS : Q_HS, slot = isk ? r : swap23(r);
        ts = isk ? REC : QT; sb = (isk ? WS_KV : WS_Q) + kt * ts;
#pragma unroll
        for (int i = 0; i < 4; ++i) { const unsigned oc = ct * 16 + 8 * wn + 2 * i + hh, hp = oc / 5, ty = oc - hp * 5; va[i] = (2 * hp + (ty > 2)) * hs + (ty > 2 ? ty - 3 : ty) * 512 + slot * 16; PIN(va[i]); }
    } else {
        sb = WS_KV + kt * REC + 1536;
#pragma unroll
        for (int i = 0; i < 2; ++i) { const unsigned c = (cg - 10) * 128 + 64 * wn + 32 * i + r, head = c / 20, d = c - head * 20; va[i] = head * KV_HS + hh * 384 + (d < 16 ? swap23(d) : d) * 16; PIN(va[i]); }
    }
    f32x16 acc[2][2];
#pragma unroll
    for (int i = 0; i < 4; ++i) { acc[i >> 1][i & 1] = zero16(); PIN(acc[i >> 1][i & 1]); }
    u32x4 xf[2][2], wf[2][2];
    auto rd_x = [&](int set, int g, int mi) { xf[set][mi] = lds128((xa ^ ((g & 3) << 5)) + mi * 4096); };
    auto rd_w = [&](int set, int g, int ni) {
        const unsigned a = wa[ni] + (g & 7) * 4096;
        const u32x2 lo = ldstr(a), hi = ldstr(a + 1024); wf[set][ni] = (u32x4){lo[0], lo[1], hi[0], hi[1]};
    };
    WAITL_BAR();
    rd_x(0, 0, 0); rd_w(0, 0, 0); rd_x(0, 0, 1); rd_w(0, 0, 1);
    unsigned xs = 0;
#pragma unroll 1
    for (int t = 0; t < 5; ++t) {
        if (PRE && __builtin_expect(t == PRE - 1, 0)) {
#pragma unroll
            for (int i = 0; i < 8; ++i) {
                const int mi = (i >> 1) & 1, ni = i >> 2, s = i & 1;
                if (T == 0) st128(rs, va[2 * ni + s], sb + mi * ts, xf[0][0]);
                else st128(rs, va[ni], sb + (mi + s) * REC + s * 768, xf[0][0]);
            }
        }
        if (__builtin_expect(t == PACE && w == 0, 0)) Flag(lane == 0, FL + b, mktag(ep)).raise(rs);
#pragma unroll
        for (int g = 0; g < 8; ++g) {
            const int c = g & 1, n = c ^ 1;
            if (c) WAITL_BAR();
            if ((g & 3) == 3) { const unsigned d = xs == 32768 ? -32768 : 16384; xs += d; xa += d; }
            SBAR();
#pragma unroll
            for (int i = 0; i < 4; ++i) {
                const int mi = i & 1, ni = i >> 1;
                acc[mi][ni] = T == 0 ? mma32(wf[c][ni], xf[c][mi], acc[mi][ni]) : mma32(xf[c][mi], wf[c][ni], acc[mi][ni]);
                if (i == 0) { rd_x(n, g + 1, 0); rd_w(n, g + 1, 0); }
                if (i == 1) { rd_x(n, g + 1, 1); rd_w(n, g + 1, 1); }
                SBAR();
            }
        }
    }
    const float sc = isk ? 1.f : QSCALE;
#pragma unroll
    for (int i = 0; i < 8; ++i) {
        const int mi = (i >> 1) & 1, ni = i >> 2, s = i & 1;
        if (T == 0) st128(rs, va[2 * ni + s], sb + mi * ts, pk8(acc[mi][ni], s, sc));
        else st128(rs, va[ni], sb + (mi + s) * REC + s * 768, pk8(acc[mi][ni], s));
    }
}

DI void qkv_load(const char* x, const char* wqkv, int li, int lane, int rb, int cg) {
    const int rot = 5 * ((rb ^ (cg >> 2)) & 1);
    if (li >= 2) {
        unsigned v0 = (lane >> 4) * 3840 + ((((lane >> 2) & 3) ^ (lane >> 4)) << 6) + (lane & 3) * 16, v1 = v0 + 14336, v2 = v0 + 2 * 14336, v3 = v0 + 3 * 14336;
        unsigned off = cg * 256 + (16 * (li - 2) + 64 * rot) * 3840, lds = 49152 + (li - 2) * 4096;
        PIN(v0); PIN(v1); PIN(v2); PIN(v3); PINS(off); PINS(lds); SBAR();
        const char* base = wqkv + off;
        unsigned slot = 0; int left = 20 - 2 * rot;
        auto issue = [&]() { dma4(v0, v1, v2, v3, base, lds + slot); base += --left ? 32 * 3840 : (32 - 640) * 3840; slot = (slot + 8192) & 32767; };
        issue(); issue();
        WAITV_BAR(4);
        issue();
#pragma unroll 1
        for (int s = 0; s < 17; ++s) { issue(); WAITV_BAR(8); }
        WAITV_BAR(4); WAITV_BAR(0); BAR();
    } else {
        const unsigned row = lane >> 3;
        unsigned v0 = swap23(row) * 1280 + (((lane & 7) ^ (row >> 1)) << 4), v1 = (v0 ^ 64) + 4 * 1280 - 1024;
        unsigned off = (rb * 128 + 64 * li) * 1280 + rot * 128, lds = li * 8192;
        PIN(v0); PIN(v1); PINS(off); PINS(lds); SBAR();
        const char* base = x + off;
        int left = 10 - rot;
        auto issue = [&]() {
#pragma unroll
            for (int i = 0; i < 4; ++i) dma2(v0, v1, base + i * 16 * 1280, lds + i * 2048);
            base += --left ? 128 : 128 - 1280; lds = lds >= 32768 ? lds - 32768 : lds + 16384;
        };
#pragma unroll 1
        for (int s = 0; s < 2; ++s) issue();
        WAITV_BAR(8);
#pragma unroll 1
        for (int s = 0; s < 8; ++s) { issue(); BAR(); WAITV_BAR(8); }
        BAR(); WAITV_BAR(0); BAR(); BAR();
    }
}

DI void gate_rows(const char* x, const char* gw, rsrc_t rs, int rb) {
    const int row = rb * 128 + (threadIdx.x >> 2), sub = threadIdx.x & 3;
    float s = 0.f;
#pragma unroll 5
    for (int i = 0; i < 20; ++i) {
        const u32x4 a = *(const u32x4*)(x + row * 1280 + (4 * i + sub) * 16), g = *(const u32x4*)(gw + (4 * i + sub) * 16);
#pragma unroll
        for (int e = 0; e < 4; ++e) s += bflo(a[e]) * bflo(g[e]) + bfhi(a[e]) * bfhi(g[e]);
    }
    s += __shfl_xor(s, 1); s += __shfl_xor(s, 2);
    if (sub == 0) st32(rs, row * 4, WS_G, __float_as_uint(1.f / (1.f + __expf(-s))));
}

DI float rowmax16(const f32x16& s) {
    float m = fmaxf(fmaxf(s[0], s[1]), s[2]);
#pragma unroll
    for (int i = 3; i < 15; i += 2) m = fmaxf(fmaxf(m, s[i]), s[i + 1]);
    return fmaxf(m, s[15]);
}
DI float halfmax(float m) { const auto t = __builtin_amdgcn_permlane32_swap(__float_as_uint(m), __float_as_uint(m), false, false); return fmaxf(__uint_as_float(t[0]), __uint_as_float(t[1])); }
DI float upper(float m) { const auto t = __builtin_amdgcn_permlane32_swap(__float_as_uint(m), __float_as_uint(m), false, false); return __uint_as_float(t[1]); }

DI void attention(char* ws, rsrc_t rs, int w, int lane, int head, int qb, u64 tag) {
    const unsigned r = lane & 31, hh = lane >> 5, kh = w >> 2, qt = qb * 8 + 2 * (w & 3), odd = head & 1;
    const unsigned kv = WS_KV + head * KV_HS, kv2 = kv - odd * KV_HS + 1024;
    unsigned l16 = lane * 16, l8 = hh ? WS_END : r * 16 + odd * 8, d0 = l16 + KV_HS, d1 = hh ? d0 : d0 - odd * KV_HS;
    unsigned vg = r < 20 ? 1536 + hh * 384 + r * 16 : WS_END, vfill = r == 20 ? 0x3f803f80u : 0u, kone = hh ? 0x3f80u : 0u;
    const char* src = ws + kv + (32 * kh + 1 + (w & 3)) * REC - KV_HS;
    unsigned dst = w * REC, kvh = kv + 32 * kh * REC, kv2h = kv2 + 32 * kh * REC;
    unsigned qa = WS_Q + head * Q_HS + (qt + 1 - kh) * QT, qa2 = qa + 1024 - odd * Q_HS;
    unsigned o1 = WS_O1 + ((qt + kh) * 32 + head) * 1024, o2 = WS_O2 + ((qt + kh) * 32 + head) * 256;
    const unsigned lb = kh * 4 * REC;
    unsigned ak1 = lb + l16, ak2 = lb + (hh ? 1536 + 22 * 16 : 1024 + r * 16 + odd * 8), av = lb + 1536 + (hh ^ (r > 27)) * 384 + min(r, 21u) * 16;
    unsigned xm = 57344 + w * 3072 + l16, xo = 57344 + (w ^ 4) * 3072 + l16;
    u32x4 p0, p1 = u32x4{}, v1 = u32x4{};
    f32x16 oa = zero16(), ob = zero16();
    float e[16]; e[12] = 0.f; e[13] = 0.f; e[14] = 0.f; e[15] = 0.f;
    PIN(l16); PIN(l8); PIN(d0); PIN(d1); PIN(vg); PIN(vfill); PIN(kone); PIN(ak1); PIN(ak2); PIN(av); PIN(xm); PIN(xo);
    PIN(p1); PIN(v1); PIN(oa); PIN(ob); PIN(e[12]); PIN(e[13]); PIN(e[14]); PIN(e[15]);
    PINS(src); PINS(dst); PINS(kvh); PINS(kv2h); PINS(qa); PINS(qa2); PINS(o1); PINS(o2);
    if (w == 4) {
        const unsigned c0 = (20 * head) >> 7, c1 = (20 * head + 19) >> 7;
        await(ws, FQ + ((lane >> 1) & 15) * 16 + 5 + 5 * (lane >> 5) + ((lane & 1) ? c1 : c0), FQ + (2 * qb + (lane >> 5)) * 16 + ((lane & 16) ? c1 : c0), tag);
    }
    BAR();
    u32x2 k2 = ld64(rs, l8, kv2h), q2a = ld64(rs, l8, qa2);
    u32x4 k1 = ld128(rs, l16, kvh);
    const u32x4 q1a = ld128(rs, l16, qa);
    SBAR();
    u32x4 v0 = ld128(rs, vg, kvh);
    dma_rec(d0, d1, src, dst);
    SBAR();
    f32x16 sa = mma32(k1, q1a, mma32k8(k2, q2a, f32x16{})), sb;
    *(LAS u32x4*)(uintptr_t)(xm + 1024) = q1a;
    unsigned na = pk2(-halfmax(rowmax16(sa)), 0.f); PIN(na);
    q2a[0] = hh ? na : q2a[0]; k2[0] |= kone;
    *(LAS u32x2*)(uintptr_t)xm = q2a;
    sa = mma32(k1, q1a, mma32k8(k2, q2a, f32x16{}));
    SBAR();
    WAITL_BAR();
    u32x2 q2b = lds64(xo);
    const u32x4 q1b = lds128(xo + 1024);
    v0 |= vfill;
    const int jl = kh ? 7 : 0;
#define EX(C, i) do { e[i] = __builtin_amdgcn_exp2f(C[i]); PIN(e[i]); SBAR(); } while (0)
#define CV(P, n, i) do { unsigned t_ = pk2(e[i], e[i + 1]); PIN(t_); P[n] = t_; SBAR(); } while (0)
#define UNIT(C, N, O, OP, Q1, Q2, u)                                                                          \
    do {                                                                                                      \
        SBAR();                                                                                               \
        N = mma32k8(k2, Q2, f32x16{}); SBAR();                                                                \
        if (u == AAT) PRIO(0);                                                                                \
        if (u == 0) {                                                                                         \
            WAITVL_BAR(); PRIO(2);                                                                            \
            if (__builtin_expect(j + jj < jl, 0)) { src += 4 * REC; dma_rec(d0, d1, src - 32 * REC, dst - 4 * REC + 32768 - fl); dma_rec(d0, d1, src, dst + 32768 - fl); } \
        }                                                                                                     \
        if (!(u & 1)) { k2 = lds64(ak2 + (u >> 1) * REC + fl); SBAR(); }                                      \
        EX(C, 0); CV(p1, 2, 12); EX(C, 1); CV(p1, 3, 14); EX(C, 2); EX(C, 3);                                 \
        OP = mma32(v1, p1, OP); SBAR();                                                                       \
        if (!(u & 1)) { v1 = lds128(av + 768 + (u >> 1) * REC + fl); SBAR(); }                                \
        EX(C, 4); CV(p0, 0, 0); EX(C, 5); CV(p0, 1, 2); EX(C, 6); EX(C, 7);                                   \
        N = mma32(k1, Q1, N); SBAR();                                                                         \
        if (!(u & 1)) { k1 = lds128(ak1 + (u >> 1) * REC + fl); SBAR(); }                                     \
        EX(C, 8); CV(p0, 2, 4); EX(C, 9); CV(p0, 3, 6); EX(C, 10); EX(C, 11);                                 \
        O = mma32(v0, p0, O); SBAR();                                                                         \
        if (u & 1) { v0 = lds128(av + (u >> 1) * REC + fl); SBAR(); }                                         \
        EX(C, 12); CV(p1, 0, 8); EX(C, 13); CV(p1, 1, 10); EX(C, 14); EX(C, 15);                              \
    } while (0)
#pragma unroll 1
    for (int j = 0; j < 8; j += 2) {
#pragma unroll
        for (int jj = 0; jj < 2; ++jj) {
            const unsigned fl = jj * 32768;
            UNIT(sa, sb, oa, ob, q1b, q2b, 0); UNIT(sb, sa, ob, oa, q1a, q2a, 1); UNIT(sa, sb, oa, ob, q1b, q2b, 2); UNIT(sb, sa, ob, oa, q1a, q2a, 3);
            UNIT(sa, sb, oa, ob, q1b, q2b, 4); UNIT(sb, sa, ob, oa, q1a, q2a, 5); UNIT(sa, sb, oa, ob, q1b, q2b, 6); UNIT(sb, sa, ob, oa, q1a, q2a, 7);
        }
    }
#undef UNIT
#undef EX
#undef CV
    LAS f32x4* mine = (LAS f32x4*)(uintptr_t)xm;
    const LAS f32x4* other = (const LAS f32x4*)(uintptr_t)xo;
    p1[2] = pk2(e[12], e[13]); p1[3] = pk2(e[14], e[15]);
    ob = mma32(v1, p1, ob);
#pragma unroll
    for (int i = 0; i < 3; ++i) mine[64 * i] = (f32x4){oa[4 * i], oa[4 * i + 1], oa[4 * i + 2], oa[4 * i + 3]};
    WAITL_BAR();
    PRIO(3);
    f32x16 o;
#pragma unroll
    for (int i = 0; i < 3; ++i) {
        const f32x4 t = other[64 * i];
#pragma unroll
        for (int c = 0; c < 4; ++c) o[4 * i + c] = ob[4 * i + c] + t[c];
    }
    float l = upper(o[8]);
    const float mag = ((fabsf(o[0]) + fabsf(o[1])) + (fabsf(o[2]) + fabsf(o[3]))) + ((fabsf(o[4]) + fabsf(o[5])) + (fabsf(o[6]) + fabsf(o[7]))) + ((fabsf(o[8]) + fabsf(o[9])) + (fabsf(o[10]) + fabsf(o[11])));
    if (__builtin_expect(__any(!(mag * 0x1p-20f + l < 0x1p100f) || !(l > 0x1p-40f)), 0)) {
        if (hh) q2b[0] = 0u;
        float m = -INFINITY;
#pragma unroll 1
        for (int t = 0; t < 64; ++t) m = fmaxf(m, rowmax16(mma32(ld128(rs, l16, kv + t * REC), q1b, mma32k8(ld64(rs, l8, kv2 + t * REC), q2b, f32x16{}))));
        m = halfmax(m);
        o = f32x16{};
#pragma unroll 1
        for (int t = 0; t < 64; ++t) {
            const u32x4 va = ld128(rs, vg, kv + t * REC) | vfill, vb = ld128(rs, vg, kv + (t + 1) * REC + 768) | vfill;
            f32x16 s = mma32(ld128(rs, l16, kv + t * REC), q1b, mma32k8(ld64(rs, l8, kv2 + t * REC), q2b, f32x16{}));
#pragma unroll
            for (int i = 0; i < 16; ++i) s[i] = __builtin_amdgcn_exp2f(s[i] - m);
            o = mma32(vb, pk8(s, 1), mma32(va, pk8(s, 0), o));
        }
        l = upper(o[8]);
    }
    const float sc = __builtin_amdgcn_rcpf(l);
    st128(rs, l16, o1, pk8(o, 0, sc));
    st64(rs, hh ? WS_END : l16 >> 1, o2, (u32x2){pk2(o[8] * sc, o[9] * sc), pk2(o[10] * sc, o[11] * sc)});
}

DI void role_a(const char* x, const char* gw, char* ws, rsrc_t rs, int w, int lane, int b, int rb, int cg) {
    const int head = (b & 7) * 4 + ((b >> 3) & 3), qb = b >> 5;
    const u64 ep = ldflag(ws, WS_E);
    if (__builtin_expect(cg == 15, 0)) { gate_rows(x, gw, rs, rb); Flag(threadIdx.x == 0, FL + b, mktag(ep)).raise(rs); }
    if (w >= 4) {
        const unsigned i = threadIdx.x - 256, rec = i >> 4, c = (i & 3) == 0 ? 0x3f803f80u : 0u;
        *(LAS u32x4*)(uintptr_t)((rec >> 3) * 32768 + (rec & 7) * REC + 1536 + ((i >> 2) & 3) * 384 + 320 + (i & 3) * 16) = (u32x4){(i & 3) == 2 ? 0x3f80u : c, c, c, c};
        const Flag fq(threadIdx.x == 320, FQ + rb * 16 + cg, mktag(ep));
        WAITVL_BAR();
        fq.raise(rs);
    } else {
        if (cg < 10) qkv_compute<0>(rs, w, lane, rb, cg, b, ep); else if (cg < 15) qkv_compute<1>(rs, w, lane, rb, cg, b, ep);
        WAITV_BAR(0);
    }
    const u64 tag = mktag(ep);
    attention(ws, rs, w, lane, head, qb, tag);
    const Flag fo(threadIdx.x == 0, FO + qb * 32 + head, tag);
    WAITV_BAR(0);
    fo.raise(rs);
    if (b == 0) {
        await(ws, FQ + threadIdx.x, tag);
        BAR();
        if (threadIdx.x == 0) stflag(ws, WS_E, tag);
    }
}

DI void hid_compute(rsrc_t rs, int w, int lane, int rb, int cb16) {
    const unsigned rr = lane & 15, g = lane >> 4, rel = rr * 128 + ((g ^ (rr >> 1)) << 4);
    unsigned xa = w * 4096 + rel, wa = 49152 + rel;
    f32x4 acc[2][5];
#pragma unroll
    for (int i = 0; i < 10; ++i) { acc[i / 5][i % 5] = f32x4{}; PIN(acc[i / 5][i % 5]); }
    PIN(xa); PIN(wa);
    u32x4 f[2][7];
    auto rd = [&](int set, int ks, int i) { f[set][i] = i < 2 ? lds128((xa ^ (ks << 6)) + i * 2048) : lds128((wa ^ (ks << 6)) + (i - 2) * 2048); };
    WAITL_BAR();
    PRIO(3);
#pragma unroll
    for (int i = 0; i < 7; ++i) rd(0, 0, i);
    unsigned sb = 0;
#pragma unroll 2
    for (int s = 0; s < 10; ++s) {
#pragma unroll
        for (int ks = 0; ks < 2; ++ks) {
            if (ks) { WAITL_BAR(); const int d = sb == 2 ? -2 : 1; sb += d; xa += d * 16384; wa += d * 10240; }
            SBAR();
#pragma unroll
            for (int i = 0; i < 10; ++i) { acc[i & 1][i >> 1] = mma16(f[ks][i & 1], f[ks][2 + (i >> 1)], acc[i & 1][i >> 1]); if (i < 7) rd(ks ^ 1, ks ^ 1, i); SBAR(); }
        }
    }
    const unsigned so = WS_AB + ((cb16 >> 1) * 64 + rb * 4 + w) * 10240 + (cb16 & 1) * 5120;
#pragma unroll
    for (int j = 0; j < 5; ++j) st128(rs, lane * 16 + (j & 3) * 1024, so + (j >> 2) * 4096, pk44(acc[0][j], acc[1][j]));
}

DI void hid_load(const char* x, const char* wd, const char* ws, int li, int lane, int rb, int cb16, int b, u64 tag) {
    const int rot = ((rb & 7) * 5) >> 2;
    const unsigned row = lane >> 3, ev = swap23(row) * 1280 + (((lane & 7) ^ (row >> 1)) << 4);
    int left = 10 - rot;
    if (li < 2) {
        const unsigned v1 = (ev ^ 64) + 16 * 1280 - 1024;
        const char* base = x + (rb * 128 + 64 * li) * 1280 + rot * 128;
        unsigned lds = li * 8192;
        auto issue = [&]() {
#pragma unroll
            for (int i = 0; i < 4; ++i) dma2(ev, v1, base + ((i >> 1) * 32 + (i & 1) * 4) * 1280, lds + i * 2048);
            base += --left ? 128 : 128 - 1280; lds = lds >= 32768 ? lds - 32768 : lds + 16384;
        };
        await<NL>(ws, FL + b, tag);
#pragma unroll 1
        for (int s = 0; s < 3; ++s) { issue(); if (s == 1) WAITV_BAR(8); }
#pragma unroll 1
        for (int s = 0; s < 7; ++s) { WAITV_BAR(8); issue(); }
        WAITV_BAR(8); WAITV_BAR(0); BAR();
    } else {
        const unsigned o = li - 2;
        unsigned vw[5];
#pragma unroll
        for (int i = 0; i < 5; ++i) { const unsigned p = 5 * o + i; vw[i] = (p < 8 ? (ev ^ ((p & 1) << 6)) + (32 * (p >> 2) + 4 * ((p >> 1) & 1) + 16 * (p & 1)) * 1280 : ((ev & 127) ^ ((p & 1) << 6)) + (8 * p + row) * 1280) + 3072 - (i & 3) * 1024; }
        const char* base = wd + (cb16 & 7) * 80 * 1280 + rot * 128 - 3072;
        unsigned lds = 49152 + o * 5120;
        auto issue = [&]() { dma4(vw[0], vw[1], vw[2], vw[3], base, lds); dma1(vw[4], base, lds + 4096); base += --left ? 128 : 128 - 1280; lds = lds >= 49152 + 20480 ? lds - 20480 : lds + 10240; };
        __builtin_amdgcn_s_sleep(32);
#pragma unroll 1
        for (int s = 0; s < 3; ++s) issue();
        WAITV_BAR(10);
#pragma unroll 1
        for (int s = 0; s < 7; ++s) { WAITV_BAR(5); issue(); }
        WAITV_BAR(5); WAITV_BAR(0); BAR();
    }
}

DI void retile_wo(const char* wo, rsrc_t rs, int f, int lane) {
    const int cbk = f / 100, st = (f - cbk * 100) / 5, tile = f % 5, kh = st < 16 ? st >> 3 : (st >> 1) & 1, i = st < 16 ? st & 7 : 8 + (st & 1);
    const int rr = lane & 15, g = lane >> 4;
    const int e = cbk * 80 + (tile < 4 ? 32 * ((tile >> 1) ^ kh) + 8 * (rr >> 2) + 4 * (tile & 1) + (rr & 3) : 64 + rr);
    const int k0 = i < 8 ? (16 * kh + 2 * i + (g >> 1)) * 20 + 8 * (g & 1) : (16 * kh + 8 * (i - 8) + 2 * g) * 20 + 16, k4 = i < 8 ? k0 + 4 : k0 + 20;
    const unsigned short* p = (const unsigned short*)wo + e;
    u32x4 v;
    v[0] = p[k0 * 640] | ((unsigned)p[(k0 + 1) * 640] << 16); v[1] = p[(k0 + 2) * 640] | ((unsigned)p[(k0 + 3) * 640] << 16);
    v[2] = p[k4 * 640] | ((unsigned)p[(k4 + 1) * 640] << 16); v[3] = p[(k4 + 2) * 640] | ((unsigned)p[(k4 + 3) * 640] << 16);
    st128(rs, lane * 16, WS_WT + f * 1024, v);
}

DI void seq_compute(rsrc_t rs, int w, int lane, int kq, int mt, int nt, int hid, u64 tag) {
    const unsigned rr = lane & 15, g = lane >> 4;
    unsigned ac[2], ab = 49152 + (w >> 1) * 5120 + lane * 16;
    ac[0] = ((w & 1) * 64 + rr) * 128 + ((g ^ (rr >> 1)) << 4); ac[1] = ac[0] ^ 64;
    PIN(ac[0]); PIN(ac[1]); PIN(ab);
    f32x4 acc[4][5];
#pragma unroll
    for (int j = 0; j < 5; ++j) {
        f32x16 z = zero16(); PIN(z);
#pragma unroll
        for (int i = 0; i < 4; ++i) acc[i][j] = (f32x4){z[4 * i], z[4 * i + 1], z[4 * i + 2], z[4 * i + 3]};
    }
    u32x4 cf[4], af[3];
    WAITVL_BAR();
    if (w == 0) Flag(lane == 0, FW + hid, tag).raise(rs);
    PRIO(0);
#pragma unroll
    for (int i = 0; i < 4; ++i) cf[i] = lds128(ac[0] + i * 2048);
    af[0] = lds128(ab); af[1] = lds128(ab + 1024);
    auto step = [&](int k) {
        const int n = (k + 1) % 6;
#pragma unroll
        for (int j = 0; j < 5; ++j) {
            if (j == 3) WAITL_BAR();
            SBAR();
            af[(k * 5 + j + 2) % 3] = lds128(ab + (j < 3 ? (k % 3) * 10240 + (j + 2) * 1024 : (n % 3) * 10240 + (j - 3) * 1024));
            SBAR();
#pragma unroll
            for (int i0 = 0; i0 < 4; ++i0) {
                const int i = (j & 1) ? 3 - i0 : i0;
                acc[i][j] = mma16(af[(k * 5 + j) % 3], cf[i], acc[i][j]);
                if (j == 4) cf[i] = lds128(ac[n & 1] + (n >> 1) * 16384 + i * 2048);
                SBAR();
            }
        }
    };
#pragma unroll 1
    for (int t = 0; t < 5; ++t) {
        if (__builtin_amdgcn_readfirstlane(t) == GSW) PRIO(1);
#pragma unroll
        for (int k = 0; k < 6; ++k) step(k);
    }
    step(0); step(1);
    PRIO(3);
    const unsigned sb = WS_P + (kq * 256 + (mt * 2 + (w & 1)) * 8 + nt * 2 + (w >> 1)) * 10240;
    auto part = [&](int i) { return i < 8 ? pk44(acc[i >> 1][2 * (i & 1)], acc[i >> 1][2 * (i & 1) + 1]) : pk44(acc[2 * (i & 1)][4], acc[2 * (i & 1) + 1][4]); };
    if (w == kq) {
#pragma unroll
        for (int i = 0; i < 10; ++i) *(LAS u32x4*)(uintptr_t)(59392 + lane * 16 + i * 1024) = part(i);
    } else {
#pragma unroll
        for (int i = 0; i < 10; ++i) st128(rs, lane * 16 + (i & 3) * 1024, sb + (i >> 2) * 4096, part(i));
    }
}

DI void seq_load(const char* c, const char* ws, int li, int lane, int kq, int mt, int nt, int cb, unsigned fw, u64 tag, rsrc_t rs, u32x4 (&wl)[10]) {
    auto frags = [&]() {
#pragma unroll
        for (int i = 0; i < 10; ++i) wl[i] = ld128(rs, lane * 16 + (i & 3) * 1024, WS_WT + (cb * 100 + 90 + (i & ~3)) * 1024);
    };
    const char* wt = ws + WS_WT + cb * 102400;
    const int rot = (nt & 1) * 8;
    if (li < 2) {
        const unsigned row = lane >> 3, v0 = row * 4096 + (((lane & 7) ^ (row >> 1)) << 4), v1 = (v0 ^ 64) + 8 * 4096 - 1024;
        const char* base = c + (size_t)(mt * 128 + 64 * li) * 4096 + (kq & 1) * 2048 + rot * 128;
        unsigned lds = li * 8192; int left = 16 - rot;
        auto next = [&]() { lds = lds >= 32768 ? lds - 32768 : lds + 16384; };
        auto issue = [&]() {
#pragma unroll
            for (int i = 0; i < 4; ++i) dma2(v0, v1, base + i * 65536, lds + i * 2048);
            base += --left ? 128 : 128 - 2048; next();
        };
#pragma unroll 1
        for (int s = 0; s < 3; ++s) { issue(); if (s == 0) WAITV_BAR(0); }
#pragma unroll 1
        for (int s = 0; s < 13; ++s) { BAR(); WAITV_BAR(8); issue(); }
        frags();
#pragma unroll 1
        for (int s = 0; s < 3; ++s) { BAR(); WAITV_BAR(8); dma4c(lane * 16, wt + lds, lds); dma4c(lane * 16, wt + lds + 4096, lds + 4096); next(); }
    } else {
        const char* bb = ws + WS_AB + (((kq >> 1) * 4 + nt) * 128 + (kq & 1) * 64 + 4 * rot + li - 2) * 5120;
        unsigned lds = 49152 + (li - 2) * 5120; int left = 32 - 2 * rot;
        auto next = [&]() { lds = lds >= 49152 + 20480 ? lds - 20480 : lds + 10240; };
        auto issue = [&]() { dma5c(lane * 16, bb, lds); bb += --left ? 10240 : 10240 - 64 * 5120; next(); };
        await<NA>(ws, FA + ((kq & 1) * 8 + (lane >> 3)) * 16 + (kq >> 1) * 8 + 2 * nt + ((lane >> 2) & 1), tag);
#pragma unroll 1
        for (int s = 0; s < 2; ++s) issue();
        WAITV_BAR(5);
        issue();
#pragma unroll 1
        for (int s = 0; s < 20; ++s) { WAITV_BAR(5); issue(); }
        await(ws, fw, tag);
        frags();
        dma1c(lane * 16, wt + 79872 + (li - 2) * 1024, 79872 + (li - 2) * 1024);
#pragma unroll 1
        for (int s = 20; s < 29; ++s) { WAITV_BAR(5); issue(); }
#pragma unroll 1
        for (int s = 0; s < 2; ++s) { WAITV_BAR(5); dma5c(lane * 16, wt + lds + (s ? 0 : 81920 - 69632), lds); next(); }
        WAITV_BAR(5);
    }
}

DI void role_g(const char* x, const char* cr, const char* ci, const char* wr, const char* wi, const char* wo, char* ws, char* out, rsrc_t rs, int w, int lane, int b) {
    u64 tag = mktag(ldflag(ws, WS_E));
    PINS(tag);
    const int xcd = b & 7, idx = b >> 3;
    const int rb = (xcd & 1) * 8 + (idx & 7), cb16 = (xcd >> 1) * 4 + (idx >> 3), hid = rb * 16 + cb16;
    const int kq = xcd >> 1, mt = (xcd & 1) * 8 + (idx >> 2), nt = idx & 3;
    const int rbk = 2 * mt + (kq & 1), cb = 2 * nt + (kq >> 1);
    if (w < 4) hid_compute(rs, w, lane, rb, cb16);
    else hid_load(x, cb16 < 8 ? wr : wi, ws, w - 4, lane, rb, cb16, b, tag);
    PRIO(3);
    const Flag fa(threadIdx.x == 0, FA + hid, tag);
    WAITVL_BAR();
    fa.raise(rs);
    u32x4 wl[10];
    if (w < 4) {
        if (hid < 200) retile_wo(wo, rs, hid * 4 + w, lane);
        seq_compute(rs, w, lane, kq, mt, nt, hid, tag);
#pragma unroll
        for (int i = 0; i < 10; ++i) wl[i] = lds128(69632 + lane * 16 + i * 1024);
    } else seq_load(kq < 2 ? cr : ci, ws, w - 4, lane, kq, mt, nt, cb, lane < 25 ? FW + 25 * cb + lane : FQ + (rbk >> 1) * 16 + 15, tag, rs, wl);
    const unsigned ts = w & 3, kh = w >> 2, g = lane >> 4, tt = rbk * 2 + (ts >> 1), tq = (ts & 1) * 16 + (lane & 15), l16 = lane * 16;
    const float gate = __uint_as_float(ld32(rs, tq * 4, WS_G + tt * 128));
    const Flag fp(threadIdx.x == 0, FP + (mt * 4 + nt) * 4 + kq, tag);
    WAITVL_BAR();
    fp.raise(rs);
    if (w >= 6) { const unsigned a = 69632 + (w - 6) * 5120; dma5c(l16, ws + WS_WT + cb * 102400 + a, a); }
    u32x4 pa[4]; u32x2 pb[4];
    pa[0] = lds128(59392 + (2 * ts + kh) * 1024 + l16); pb[0] = lds64(59392 + (8 + (ts >> 1)) * 1024 + (ts & 1) * 8 + l16);
    if (w == 4) await<NA>(ws, FP + (mt * 4 + nt) * 4 + (lane >> 4), tag);
    WAITL_BAR();
    if (w >= 6) { const unsigned a = 59392 + (w - 6) * 5120; dma5c(l16, ws + WS_WT + cb * 102400 + a, a); }
    const unsigned ps = WS_P + (rbk * 8 + cb) * 10240;
#pragma unroll
    for (int q = 1; q < 4; ++q) { pa[q] = ld128(rs, l16, ps + (q ^ kq) * (256 * 10240) + (2 * ts + kh) * 1024); pb[q] = ld64(rs, l16, kh ? WS_END : ps + (q ^ kq) * (256 * 10240) + (8 + (ts >> 1)) * 1024 + (ts & 1) * 8); }
    SBAR();
    PRIO(0);
    SBAR();
    float y[12];
#pragma unroll
    for (int e = 0; e < 4; ++e) { y[2 * e] = (bflo(pa[0][e]) + bflo(pa[1][e])) - (bflo(pa[2][e]) + bflo(pa[3][e])); y[2 * e + 1] = (bfhi(pa[0][e]) + bfhi(pa[1][e])) - (bfhi(pa[2][e]) + bfhi(pa[3][e])); }
#pragma unroll
    for (int e = 0; e < 2; ++e) { y[8 + 2 * e] = (bflo(pb[0][e]) + bflo(pb[1][e])) - (bflo(pb[2][e]) + bflo(pb[3][e])); y[9 + 2 * e] = (bfhi(pb[0][e]) + bfhi(pb[1][e])) - (bfhi(pb[2][e]) + bfhi(pb[3][e])); }
#pragma unroll
    for (int i = 0; i < 12; ++i) { y[i] *= kq < 2 ? gate : -gate; PIN(y[i]); }
    float og = 1.f - gate;
    unsigned aw = kh * 40960 + l16, vo = g * 512 + tq * 16, vo2 = g * 512 + tq * 8;
    unsigned oa = (tt * 32 + (ts & 1) * 16 + (lane >> 2)) * 1280 + cb * 160 + kh * 64 + (lane & 3) * 16, ob = kh ? 2048 * 1280 : oa + 128 - (lane & 3) * 8, op = (lane & 3) * 64 + (lane & ~3);
    unsigned o1[4], o2 = WS_O2 + (tt * 32 + 16 * kh) * 256;
#pragma unroll
    for (int i = 0; i < 4; ++i) { o1[i] = WS_O1 + (tt * 32 + 16 * kh) * 1024 + i * 4096; PINS(o1[i]); }
    PIN(og); PIN(oa); PIN(ob); PIN(op); PIN(aw); PIN(vo); PIN(vo2); PINS(o2);
    if (w == 4) await<NO>(ws, FO + (rbk >> 2) * 32 + (lane >> 1), tag);
    WAITV_BAR(0);
    u32x4 of[10];
#pragma unroll
    for (int i = 0; i < 2; ++i) { const u32x2 lo = ld64(rs, vo2 + i * 2048, o2), hi = ld64(rs, vo2 + i * 2048 + 256, o2); of[i] = (u32x4){lo[0], lo[1], hi[0], hi[1]}; }
#pragma unroll
    for (int i = 0; i < 8; ++i) of[2 + i] = ld128(rs, vo + (i & 1) * 2048, o1[i >> 1]);
    SBAR();
    PRIO(3);
    f32x4 acc[5];
#pragma unroll
    for (int n = 0; n < 50; ++n) {
        acc[n % 5] = mma16(wl[n % 10], of[n / 5], n < 5 ? f32x4{} : acc[n % 5]);
        if (n < 40) wl[n % 10] = lds128(aw + n * 1024);
        if (n == 44) BAR();
        SBAR();
    }
    LAS f32x4* mine = (LAS f32x4*)(uintptr_t)(aw + ts * 3072);
    const LAS f32x4* other = (const LAS f32x4*)(uintptr_t)((aw ^ 40960) + ts * 3072);
    mine[0] = acc[2]; mine[64] = acc[3]; mine[128] = acc[4];
    WAITL_BAR();
    const f32x4 a0 = acc[0] + other[0], a1 = acc[1] + other[64], a2 = acc[4] + other[128];
    u32x4 r0 = {pk2(og * a0[0] + y[0], og * a0[1] + y[1]), pk2(og * a0[2] + y[2], og * a0[3] + y[3]), pk2(og * a1[0] + y[4], og * a1[1] + y[5]), pk2(og * a1[2] + y[6], og * a1[3] + y[7])};
    u32x2 r1 = {pk2(og * a2[0] + y[8], og * a2[1] + y[9]), pk2(og * a2[2] + y[10], og * a2[3] + y[11])};
#pragma unroll
    for (int i = 0; i < 4; ++i) r0[i] = __builtin_amdgcn_ds_bpermute(op, r0[i]);
#pragma unroll
    for (int i = 0; i < 2; ++i) r1[i] = __builtin_amdgcn_ds_bpermute(op, r1[i]);
    const rsrc_t ro = __builtin_amdgcn_make_buffer_rsrc(out, 0, 2048 * 1280, 0x00020000);
    __builtin_amdgcn_raw_buffer_store_b128(r0, ro, oa, 0, 0);
    __builtin_amdgcn_raw_buffer_store_b64(r1, ro, ob, 0, 0);
}

__global__ __launch_bounds__(512, 4) void fused_mix(const char* x, const char* cr, const char* ci, const char* wr, const char* wi, const char* wqkv, const char* wo, const char* gw, char* ws, char* out) {
    const int lane = threadIdx.x & 63, w = __builtin_amdgcn_readfirstlane(threadIdx.x >> 6);
    const rsrc_t rs = __builtin_amdgcn_make_buffer_rsrc(ws, 0, WS_END, 0x00020000);
    const int b = blockIdx.x, rb = (b & 1) * 8 + ((b >> 3) & 7), cg = ((b >> 1) & 3) + 4 * (b >> 6);
    if ((b >> 3) == 32 && w < 3) {
        const char* pc; asm volatile("s_getpc_b64 %0" : "=s"(pc));
        const unsigned t = *(const GAS unsigned*)(pc + min((unsigned)w * 8192 + lane * 128, CODE_BYTES));
        asm volatile("" :: "v"(t));
    }
    if (w >= 4 && cg < 15) { PRIO(3); qkv_load(x, wqkv, w - 4, lane, rb, cg); }
    if (b >= 256) { PRIO(2); role_g(x, cr, ci, wr, wi, wo, ws, out, rs, w, lane, b - 256); }
    else { PRIO(3); role_a(x, gw, ws, rs, w, lane, b, rb, cg); }
}

extern "C" void kernel_launch(void* const* d_in, const int* in_sizes, int n_in, void* d_out, int out_size, void* d_ws, size_t ws_size, hipStream_t stream) {
    (void)hipFuncSetAttribute((const void*)fused_mix, hipFuncAttributeMaxDynamicSharedMemorySize, LDS_BYTES);
    fused_mix<<<512, 512, LDS_BYTES, stream>>>((const char*)d_in[0], (const char*)d_in[1], (const char*)d_in[2], (const char*)d_in[3], (const char*)d_in[4],
                                               (const char*)d_in[6], (const char*)d_in[7], (const char*)d_in[8], (char*)d_ws, (char*)d_out);
}
```

```cpp
#include <hip/hip_runtime.h>
#include <hip/hip_bf16.h>
#include <stdint.h>

typedef __attribute__((ext_vector_type(8))) __bf16 bf16x8;
typedef __attribute__((ext_vector_type(2))) __bf16 bf16x2;
typedef __attribute__((ext_vector_type(4))) short s16x4;
typedef __attribute__((ext_vector_type(16))) float f32x16;
typedef __attribute__((ext_vector_type(4))) float f32x4;
typedef __attribute__((ext_vector_type(2))) float f32x2;
typedef __attribute__((ext_vector_type(4))) unsigned u32x4;
typedef __attribute__((ext_vector_type(2))) unsigned u32x2;
typedef unsigned long long u64;
typedef __amdgpu_buffer_rsrc_t rsrc_t;
#define LAS __attribute__((address_space(3)))
#define GAS __attribute__((address_space(1)))
#define DI __device__ __forceinline__
#define SBAR() __builtin_amdgcn_sched_barrier(0)
#define PIN(x) asm volatile("" : "+v"(x))
#define PINS(x) asm volatile("" : "+s"(x))
#define PRIO(n) __builtin_amdgcn_s_setprio(n)

constexpr unsigned REC = 3072, KV_HS = 65 * REC, QT = 1536, Q_HS = 64 * QT;
constexpr unsigned WS_KV = 0;
constexpr unsigned WS_Q = WS_KV + 32 * KV_HS;
constexpr unsigned WS_AB = WS_Q + 32 * Q_HS;
constexpr unsigned WS_P = WS_AB + 8 * 64 * 10240;
constexpr unsigned WS_O1 = WS_P + 4 * 256 * 10240;
constexpr unsigned WS_O2 = WS_O1 + 64 * 32 * 1024;
constexpr unsigned WS_WT = WS_O2 + 64 * 32 * 256;
constexpr unsigned WS_G = WS_WT + 800 * 1024;
constexpr unsigned WS_F = WS_G + 8192;
constexpr unsigned WS_E = WS_F + 6 * 256 * 128;
constexpr unsigned WS_END = WS_E + 128;
constexpr unsigned FQ = 0, FA = 256, FP = 512, FW = 768, FL = 1024, FO = 1280;
constexpr unsigned LDS_BYTES = 81920, CODE_BYTES = 25600;
constexpr float QSCALE = 0.22360679774997896f * 1.4426950408889634f;

DI unsigned swap23(unsigned r) { return (r & ~12u) | ((r & 4u) << 1) | ((r & 8u) >> 1); }
DI unsigned pk2(float a, float b) { f32x2 v = {a, b}; return __builtin_bit_cast(unsigned, __builtin_convertvector(v, bf16x2)); }
DI float bflo(unsigned u) { return __uint_as_float(u << 16); }
DI float bfhi(unsigned u) { return __uint_as_float(u & 0xffff0000u); }
DI f32x16 mma32(u32x4 a, u32x4 b, f32x16 c) { return __builtin_amdgcn_mfma_f32_32x32x16_bf16(__builtin_bit_cast(bf16x8, a), __builtin_bit_cast(bf16x8, b), c, 0, 0, 0); }
DI f32x16 mma32k8(u32x2 a, u32x2 b, f32x16 c) { return __builtin_amdgcn_mfma_f32_32x32x8bf16_1k(__builtin_bit_cast(s16x4, a), __builtin_bit_cast(s16x4, b), c, 0, 0, 0); }
DI f32x4 mma16(u32x4 a, u32x4 b, f32x4 c) { return __builtin_amdgcn_mfma_f32_16x16x32_bf16(__builtin_bit_cast(bf16x8, a), __builtin_bit_cast(bf16x8, b), c, 0, 0, 0); }
DI u32x4 pk8(const f32x16& a, int s, float c) { return (u32x4){pk2(a[8 * s] * c, a[8 * s + 1] * c), pk2(a[8 * s + 2] * c, a[8 * s + 3] * c), pk2(a[8 * s + 4] * c, a[8 * s + 5] * c), pk2(a[8 * s + 6] * c, a[8 * s + 7] * c)}; }
DI u32x4 pk8(const f32x16& a, int s) { return (u32x4){pk2(a[8 * s], a[8 * s + 1]), pk2(a[8 * s + 2], a[8 * s + 3]), pk2(a[8 * s + 4], a[8 * s + 5]), pk2(a[8 * s + 6], a[8 * s + 7])}; }
DI u32x4 pk44(f32x4 a, f32x4 b) { return (u32x4){pk2(a[0], a[1]), pk2(a[2], a[3]), pk2(b[0], b[1]), pk2(b[2], b[3])}; }
DI f32x16 zero16() { u32x2 z = {0u, 0u}; PIN(z); return mma32k8(z, z, f32x16{}); }
DI u32x4 lds128(unsigned a) { return *(const LAS u32x4*)(uintptr_t)a; }
DI u32x2 lds64(unsigned a) { return *(const LAS u32x2*)(uintptr_t)a; }
DI u32x2 ldstr(unsigned a) { return __builtin_bit_cast(u32x2, __builtin_amdgcn_ds_read_tr16_b64_v4i16((LAS s16x4*)(uintptr_t)a)); }
DI void st128(rsrc_t rs, unsigned v, unsigned s, u32x4 d) { __builtin_amdgcn_raw_buffer_store_b128(d, rs, v, s, 16); asm volatile("s_nop 1" :: "v"(d) : "memory"); }
DI void st64(rsrc_t rs, unsigned v, unsigned s, u32x2 d) { __builtin_amdgcn_raw_buffer_store_b64(d, rs, v, s, 16); }
DI void st32(rsrc_t rs, unsigned v, unsigned s, unsigned d) { __builtin_amdgcn_raw_buffer_store_b32(d, rs, v, s, 16); }
DI u32x4 ld128(rsrc_t rs, unsigned v, unsigned s) { return __builtin_amdgcn_raw_buffer_load_b128(rs, v, s, 16); }
DI u32x2 ld64(rsrc_t rs, unsigned v, unsigned s) { return __builtin_amdgcn_raw_buffer_load_b64(rs, v, s, 16); }
DI unsigned ld32(rsrc_t rs, unsigned v, unsigned s) { return __builtin_amdgcn_raw_buffer_load_b32(rs, v, s, 16); }
#define M0SET "s_mov_b32 m0, %[l]\n\ts_nop 0\n\t"
#define GLD(v, o, c) "global_load_lds_dwordx4 %[" v "], %[b] offset:" o c "\n\t"
DI void dma1(unsigned v0, const char* base, unsigned lds) { asm volatile(M0SET GLD("v0", "0", "") ::[v0] "v"(v0), [b] "s"(base), [l] "s"(lds) : "memory"); }
DI void dma2(unsigned v0, unsigned v1, const char* base, unsigned lds) { asm volatile(M0SET GLD("v0", "0", "") GLD("v1", "1024", "") ::[v0] "v"(v0), [v1] "v"(v1), [b] "s"(base), [l] "s"(lds) : "memory"); }
DI void dma4(unsigned v0, unsigned v1, unsigned v2, unsigned v3, const char* base, unsigned lds) {
    asm volatile(M0SET GLD("v0", "0", "") GLD("v1", "1024", "") GLD("v2", "2048", "") GLD("v3", "3072", "") ::[v0] "v"(v0), [v1] "v"(v1), [v2] "v"(v2), [v3] "v"(v3), [b] "s"(base), [l] "s"(lds) : "memory");
}
DI void dma1c(unsigned v0, const char* base, unsigned lds) { asm volatile(M0SET GLD("v0", "0", " sc1") ::[v0] "v"(v0), [b] "s"(base), [l] "s"(lds) : "memory"); }
DI void dma4c(unsigned v0, const char* base, unsigned lds) {
    asm volatile(M0SET GLD("v0", "0", " sc1") GLD("v0", "1024", " sc1") GLD("v0", "2048", " sc1") GLD("v0", "3072", " sc1") ::[v0] "v"(v0), [b] "s"(base), [l] "s"(lds) : "memory");
}
DI void dma5c(unsigned v0, const char* base, unsigned lds) { dma4c(v0, base, lds); dma1c(v0, base + 4096, lds + 4096); }
#define RECDMA(v0, v1, b, l) "s_mov_b32 m0, " l "\n\ts_nop 0\n\tglobal_load_lds_dwordx4 " v0 ", " b " sc1\n\ts_mov_b32 exec_hi, 0xff0fffff\n\tglobal_load_lds_dwordx4 " v1 ", " b " offset:1024 sc1\n\t" \
    "s_mov_b32 exec_lo, 0xffff0fff\n\ts_mov_b32 exec_hi, 0x0fffff0f\n\tglobal_load_lds_dwordx4 " v0 ", " b " offset:2048 sc1\n\ts_mov_b64 exec, -1\n\t"
DI void dma_rec(unsigned v0, unsigned v1, const char* base, unsigned lds) { asm volatile(RECDMA("%0", "%1", "%2", "%3") :: "v"(v0), "v"(v1), "s"(base), "s"(lds) : "memory"); }
#define BAR() asm volatile("s_barrier" ::: "memory")
#define WAITV_BAR(N) asm volatile("s_waitcnt vmcnt(" #N ")\n\ts_barrier" ::: "memory")
#define WAITL_BAR() asm volatile("s_waitcnt lgkmcnt(0)\n\ts_barrier" ::: "memory")
#define WAITVL_BAR() asm volatile("s_waitcnt vmcnt(0) lgkmcnt(0)\n\ts_barrier" ::: "memory")

DI u64 ldflag(const char* ws, unsigned off) { return __hip_atomic_load((const GAS u64*)(ws + off), __ATOMIC_RELAXED, __HIP_MEMORY_SCOPE_AGENT); }
DI void stflag(char* ws, unsigned off, u64 v) { __hip_atomic_store((GAS u64*)(ws + off), v, __ATOMIC_RELAXED, __HIP_MEMORY_SCOPE_AGENT); }
DI u64 mktag(u64 e) {
    const u64 u = ((u64)(unsigned)__builtin_amdgcn_readfirstlane((unsigned)(e >> 32)) << 32) | (unsigned)__builtin_amdgcn_readfirstlane((unsigned)e);
    return u * 6364136223846793005ull + 1442695040888963407ull;
}
struct Flag {
    unsigned v; u32x2 d;
    DI Flag(bool mine, unsigned f, u64 tag) { v = mine ? WS_F + f * 128 : WS_END; d = (u32x2){(unsigned)tag, (unsigned)(tag >> 32)}; PIN(v); PIN(d); }
    DI void raise(rsrc_t rs) const { st64(rs, v, 0, d); }
};
template <int NAP = 1> DI void await(const char* ws, unsigned f0, u64 tag) {
    u64 a; unsigned n = 1u << 19;
    asm volatile("1:\n\tglobal_load_dwordx2 %0, %2, %3 sc1\n\ts_sub_u32 %1, %1, 1\n\ts_sleep %c5\n\ts_waitcnt vmcnt(0)\n\tv_cmp_ne_u64 vcc, %4, %0\n\ts_cbranch_scc1 2f\n\ts_cbranch_vccnz 1b\n2:"
                 : "=&v"(a), "+s"(n) : "v"(WS_F + f0 * 128), "s"(ws), "s"(tag), "i"(NAP) : "vcc", "scc", "memory");
}
DI void await(const char* ws, unsigned f0, unsigned f1, u64 tag) {
    u64 a, b, m; unsigned n = 1u << 19;
    asm volatile("1:\n\tglobal_load_dwordx2 %0, %4, %6 sc1\n\tglobal_load_dwordx2 %1, %5, %6 sc1\n\ts_sub_u32 %3, %3, 1\n\ts_cbranch_scc1 2f\n\ts_waitcnt vmcnt(0)\n\t"
                 "v_cmp_ne_u64 %2, %7, %0\n\tv_cmp_ne_u64 vcc, %7, %1\n\ts_or_b64 vcc, vcc, %2\n\ts_cbranch_scc1 1b\n2:\n\ts_waitcnt vmcnt(0)"
                 : "=&v"(a), "=&v"(b), "=&s"(m), "+s"(n) : "v"(WS_F + f0 * 128), "v"(WS_F + f1 * 128), "s"(ws), "s"(tag) : "vcc", "scc", "memory");
}

template <int T>
DI void qkv_compute(rsrc_t rs, int w, int lane, int rb, int cg, int b, u64 ep) {
    const unsigned p = lane & 15, g = lane >> 4, wm = w & 1, wn = w >> 1;
    unsigned xa = (64 * wm + p) * 128 + ((g ^ (p >> 1)) << 4), wa[4];
#pragma unroll
    for (int j = 0; j < 4; ++j) { wa[j] = 49152 + (8 * g + (p >> 2)) * 256 + (((4 * wn + j) ^ (2 * (p >> 2) + (g & 1))) << 5) + (p & 3) * 8; PIN(wa[j]); }
    PIN(xa);
    const unsigned kt = rb * 4 + 2 * wm;
    const bool isk = cg >= 5;
    unsigned va[4], sb, ts = REC;
    if (T == 0) {
        const unsigned ct = isk ? cg - 5 : cg, hs = isk ? KV_HS : Q_HS, slot = isk ? p : swap23(p);
        ts = isk ? REC : QT; sb = (isk ? WS_KV : WS_Q) + kt * ts;
#pragma unroll
        for (int m = 0; m < 2; ++m) { const unsigned oc = ct * 16 + 8 * wn + 4 * m + (g & 1) * 2 + (g >> 1), hp = oc / 5, ty = oc - hp * 5; va[m] = (2 * hp + (ty > 2)) * hs + (ty > 2 ? ty - 3 : ty) * 512 + slot * 16; PIN(va[m]); }
    } else {
        sb = WS_KV + kt * REC + 1536;
#pragma unroll
        for (int j = 0; j < 4; ++j) { const unsigned c = (cg - 10) * 128 + 64 * wn + 16 * j + p, head = c / 20, d = c - head * 20; va[j] = head * KV_HS + (g & 1) * 384 + (g >> 1) * (768 + REC) + (d < 16 ? swap23(d) : d) * 16; PIN(va[j]); }
    }
    f32x4 acc[4][4];
#pragma unroll
    for (int j = 0; j < 4; ++j) {
        f32x16 z = zero16(); PIN(z);
#pragma unroll
        for (int i = 0; i < 4; ++i) acc[i][j] = (f32x4){z[4 * i], z[4 * i + 1], z[4 * i + 2], z[4 * i + 3]};
    }
    u32x4 xf[2][4], wf[4];
    auto rd_x = [&](int k, int i) { xf[k & 1][i] = lds128((xa ^ ((k & 1) << 6)) + i * 2048); };
    auto rd_w = [&](int k, int j) { const u32x2 lo = ldstr(wa[j] + (k & 3) * 8192), hi = ldstr(wa[j] + (k & 3) * 8192 + 1024); wf[j] = (u32x4){lo[0], lo[1], hi[0], hi[1]}; };
    WAITL_BAR();
    rd_w(0, 0); rd_x(0, 0); rd_x(0, 1); rd_x(0, 2); rd_x(0, 3); rd_w(0, 1);
    unsigned xs = 0;
#pragma unroll 1
    for (int t = 0; t < 5; ++t) {
        if (__builtin_expect(t == 3 && w == 0, 0)) Flag(threadIdx.x == 0, FL + b, mktag(ep)).raise(rs);
#pragma unroll
        for (int k = 0; k < 4; ++k) {
#pragma unroll
            for (int j = 0; j < 4; ++j) {
                if (j == 2) { WAITL_BAR(); if (k & 1) { const unsigned d = xs == 32768 ? -32768 : 16384; xs += d; xa += d; } }
                SBAR();
#pragma unroll
                for (int i0 = 0; i0 < 4; ++i0) {
                    const int i = (j & 1) ? 3 - i0 : i0;
                    acc[i][j] = T == 0 ? mma16(wf[j], xf[k & 1][i], acc[i][j]) : mma16(xf[k & 1][i], wf[j], acc[i][j]);
                    SBAR();
                    if (j == 0 && i0 < 2) rd_w(k, 2 + i0);
                    if (j == 2 && i0 == 0) rd_w(k + 1, 0);
                    if (j == 2 && i0 > 0) rd_x(k + 1, i0 - 1);
                    if (j == 3 && i0 == 0) rd_x(k + 1, 3);
                    if (j == 3 && i0 == 1) rd_w(k + 1, 1);
                    SBAR();
                }
            }
        }
    }
    const float sc = isk ? 1.f : QSCALE;
#pragma unroll
    for (int n = 0; n < 8; ++n) {
        if (T == 0) {
            const int m = n >> 2, i = n & 3;
            const u32x4 v = pk44(acc[i][2 * m] * sc, acc[i][2 * m + 1] * sc);
            const auto lo = __builtin_amdgcn_permlane16_swap(v[0], v[2], false, false), hi = __builtin_amdgcn_permlane16_swap(v[1], v[3], false, false);
            st128(rs, va[m] + (i & 1) * 256, sb + (i >> 1) * ts, (u32x4){lo[0], hi[0], lo[1], hi[1]});
        } else st128(rs, va[n >> 1], sb + (n & 1) * REC, pk44(acc[2 * (n & 1)][n >> 1], acc[2 * (n & 1) + 1][n >> 1]));
    }
}

DI void qkv_load(const char* x, const char* wqkv, int li, int lane, int rb, int cg) {
    const int rot = 5 * ((rb ^ (cg >> 2)) & 1);
    if (li >= 2) {
        unsigned v0 = (lane >> 4) * 3840 + ((((lane >> 2) & 3) ^ (lane >> 4)) << 6) + (lane & 3) * 16, v1 = v0 + 14336, v2 = (v0 ^ 32) + 2 * 14336, v3 = v2 + 14336;
        unsigned off = cg * 256 + (16 * (li - 2) + 64 * rot) * 3840, lds = 49152 + (li - 2) * 4096;
        PIN(v0); PIN(v1); PIN(v2); PIN(v3); PINS(off); PINS(lds); SBAR();
        const char* base = wqkv + off;
        unsigned slot = 0; int left = 20 - 2 * rot;
        auto issue = [&]() { dma4(v0, v1, v2, v3, base, lds + slot); base += --left ? 32 * 3840 : (32 - 640) * 3840; slot = (slot + 8192) & 32767; };
        issue(); issue();
        WAITV_BAR(4);
        issue();
#pragma unroll 1
        for (int s = 0; s < 17; ++s) { issue(); WAITV_BAR(8); }
        WAITV_BAR(4); WAITV_BAR(0); BAR();
    } else {
        const unsigned row = lane >> 3;
        unsigned v0 = swap23(row) * 1280 + (((lane & 7) ^ (row >> 1)) << 4), v1 = (v0 ^ 64) + (cg < 10 ? 4 : 16) * 1280 - 1024;
        unsigned off = (rb * 128 + 64 * li) * 1280 + rot * 128, lds = li * 8192, s1 = (cg < 10 ? 16 : 4) * 1280;
        PIN(v0); PIN(v1); PINS(off); PINS(lds); PINS(s1); SBAR();
        const char* base = x + off;
        int left = 10 - rot;
        auto issue = [&]() {
#pragma unroll
            for (int i = 0; i < 4; ++i) dma2(v0, v1, base + (i >> 1) * 32 * 1280 + (i & 1) * s1, lds + i * 2048);
            base += --left ? 128 : 128 - 1280; lds = lds >= 32768 ? lds - 32768 : lds + 16384;
        };
#pragma unroll 1
        for (int s = 0; s < 2; ++s) issue();
        WAITV_BAR(8);
#pragma unroll 1
        for (int s = 0; s < 8; ++s) { issue(); BAR(); WAITV_BAR(8); }
        BAR(); WAITV_BAR(0); BAR(); BAR();
    }
}

DI void gate_rows(const char* x, const char* gw, rsrc_t rs, int rb) {
    const int row = rb * 128 + (threadIdx.x >> 2), sub = threadIdx.x & 3;
    float s = 0.f;
#pragma unroll 5
    for (int i = 0; i < 20; ++i) {
        const u32x4 a = *(const u32x4*)(x + row * 1280 + (4 * i + sub) * 16), g = *(const u32x4*)(gw + (4 * i + sub) * 16);
#pragma unroll
        for (int e = 0; e < 4; ++e) s += bflo(a[e]) * bflo(g[e]) + bfhi(a[e]) * bfhi(g[e]);
    }
    s += __shfl_xor(s, 1); s += __shfl_xor(s, 2);
    if (sub == 0) st32(rs, row * 4, WS_G, __float_as_uint(1.f / (1.f + __expf(-s))));
}

DI float rowmax16(const f32x16& s) {
    float m = fmaxf(fmaxf(s[0], s[1]), s[2]);
#pragma unroll
    for (int i = 3; i < 15; i += 2) m = fmaxf(fmaxf(m, s[i]), s[i + 1]);
    return fmaxf(m, s[15]);
}
DI float halfmax(float m) { const auto t = __builtin_amdgcn_permlane32_swap(__float_as_uint(m), __float_as_uint(m), false, false); return fmaxf(__uint_as_float(t[0]), __uint_as_float(t[1])); }
DI float upper(float m) { const auto t = __builtin_amdgcn_permlane32_swap(__float_as_uint(m), __float_as_uint(m), false, false); return __uint_as_float(t[1]); }

DI void attention(char* ws, rsrc_t rs, int w, int lane, int head, int qb, u64 tag) {
    const unsigned r = lane & 31, hh = lane >> 5, kh = w >> 2, qt = qb * 8 + 2 * (w & 3), odd = head & 1;
    const unsigned kv = WS_KV + head * KV_HS, kv2 = kv - odd * KV_HS + 1024;
    unsigned l16 = lane * 16, l8 = hh ? WS_END : r * 16 + odd * 8, d0 = l16 + KV_HS, d1 = hh ? d0 : d0 - odd * KV_HS;
    unsigned vg = r < 20 ? 1536 + hh * 384 + r * 16 : WS_END, vfill = r == 20 ? 0x3f803f80u : 0u, kone = hh ? 0x3f80u : 0u;
    const char* src = ws + kv + (32 * kh + 1 + (w & 3)) * REC - KV_HS;
    unsigned dst = w * REC, kvh = kv + 32 * kh * REC, kv2h = kv2 + 32 * kh * REC;
    unsigned qa = WS_Q + head * Q_HS + (qt + 1 - kh) * QT, qa2 = qa + 1024 - odd * Q_HS;
    unsigned o1 = WS_O1 + ((qt + kh) * 32 + head) * 1024, o2 = WS_O2 + ((qt + kh) * 32 + head) * 256;
    const unsigned lb = kh * 4 * REC;
    unsigned ak1 = lb + l16, ak2 = lb + (hh ? 1536 + 22 * 16 : 1024 + r * 16 + odd * 8), av = lb + 1536 + (hh ^ (r > 27)) * 384 + min(r, 21u) * 16;
    unsigned xm = 57344 + w * 3072 + l16, xo = 57344 + (w ^ 4) * 3072 + l16;
    u32x4 p0, p1 = u32x4{}, v1 = u32x4{};
    f32x16 oa = zero16(), ob = zero16();
    float e[16]; e[12] = 0.f; e[13] = 0.f; e[14] = 0.f; e[15] = 0.f;
    PIN(l16); PIN(l8); PIN(d0); PIN(d1); PIN(vg); PIN(vfill); PIN(kone); PIN(ak1); PIN(ak2); PIN(av); PIN(xm); PIN(xo);
    PIN(p1); PIN(v1); PIN(oa); PIN(ob); PIN(e[12]); PIN(e[13]); PIN(e[14]); PIN(e[15]);
    u32x4 desc = {(unsigned)(uintptr_t)ws, (unsigned)((uintptr_t)ws >> 32) & 0xffffu, WS_END, 0x00020000u};
    const char* src0 = src - 32 * REC; unsigned dst0 = dst - 4 * REC;
    PINS(desc); PINS(src0); PINS(dst0); PINS(src); PINS(dst); PINS(kvh); PINS(kv2h); PINS(qa); PINS(qa2); PINS(o1); PINS(o2);
    if (w == 4) {
        const unsigned c0 = (20 * head) >> 7, c1 = (20 * head + 19) >> 7;
        await(ws, FQ + ((lane >> 1) & 15) * 16 + 5 + 5 * (lane >> 5) + ((lane & 1) ? c1 : c0), FQ + (2 * qb + (lane >> 5)) * 16 + ((lane & 16) ? c1 : c0), tag);
    }
    BAR();
    u32x2 k2, q2a; u32x4 k1, q1a, v0;
    asm volatile("buffer_load_dwordx2 %0, %5, %8, %9 offen sc1\n\tbuffer_load_dwordx2 %1, %5, %8, %10 offen sc1\n\tbuffer_load_dwordx4 %2, %6, %8, %11 offen sc1\n\t"
                 "buffer_load_dwordx4 %3, %6, %8, %12 offen sc1\n\tbuffer_load_dwordx4 %4, %7, %8, %11 offen sc1\n\t"
                 "s_cmp_eq_u32 %13, 0\n\ts_cbranch_scc1 1f\n\t" RECDMA("%14", "%15", "%16", "%18") RECDMA("%14", "%15", "%17", "%19") "s_waitcnt vmcnt(6)\n\ts_branch 2f\n1:\n\ts_waitcnt vmcnt(0)\n2:"
                 : "=&v"(k2), "=&v"(q2a), "=&v"(k1), "=&v"(q1a), "=&v"(v0)
                 : "v"(l8), "v"(l16), "v"(vg), "s"(desc), "s"(kv2h), "s"(qa2), "s"(kvh), "s"(qa), "s"(kh), "v"(d0), "v"(d1), "s"(src0), "s"(src), "s"(dst0), "s"(dst) : "scc", "memory");
    f32x16 sa = mma32(k1, q1a, mma32k8(k2, q2a, f32x16{})), sb;
    *(LAS u32x4*)(uintptr_t)(xm + 1024) = q1a;
    unsigned na = pk2(-halfmax(rowmax16(sa)), 0.f); PIN(na);
    q2a[0] = hh ? na : q2a[0]; k2[0] |= kone; v0 |= vfill; PIN(v0);
    *(LAS u32x2*)(uintptr_t)xm = q2a;
    sa = mma32(k1, q1a, mma32k8(k2, q2a, f32x16{}));
    SBAR();
    WAITL_BAR();
    u32x2 q2b = lds64(xo);
    const u32x4 q1b = lds128(xo + 1024);
    const int jl = kh ? 7 : 0;
#define EX(C, i) do { e[i] = __builtin_amdgcn_exp2f(C[i]); PIN(e[i]); SBAR(); } while (0)
#define CV(P, n, i) do { unsigned t_ = pk2(e[i], e[i + 1]); PIN(t_); P[n] = t_; SBAR(); } while (0)
#define UNIT(C, N, O, OP, Q1, Q2, u)                                                                          \
    do {                                                                                                      \
        SBAR();                                                                                               \
        N = mma32k8(k2, Q2, f32x16{}); SBAR();                                                                \
        if (u == 6) PRIO(0);                                                                                  \
        if (u == 0) {                                                                                         \
            WAITVL_BAR(); PRIO(2);                                                                            \
            if (__builtin_expect(j + jj < jl, 0)) { src += 4 * REC; dma_rec(d0, d1, src - 32 * REC, dst - 4 * REC + 32768 - fl); dma_rec(d0, d1, src, dst + 32768 - fl); } \
        }                                                                                                     \
        if (!(u & 1)) { k2 = lds64(ak2 + (u >> 1) * REC + fl); SBAR(); }                                      \
        EX(C, 0); CV(p1, 2, 12); EX(C, 1); CV(p1, 3, 14); EX(C, 2); EX(C, 3);                                 \
        OP = mma32(v1, p1, OP); SBAR();                                                                       \
        if (!(u & 1)) { v1 = lds128(av + 768 + (u >> 1) * REC + fl); SBAR(); }                                \
        EX(C, 4); CV(p0, 0, 0); EX(C, 5); CV(p0, 1, 2); EX(C, 6); EX(C, 7);                                   \
        N = mma32(k1, Q1, N); SBAR();                                                                         \
        if (!(u & 1)) { k1 = lds128(ak1 + (u >> 1) * REC + fl); SBAR(); }                                     \
        EX(C, 8); CV(p0, 2, 4); EX(C, 9); CV(p0, 3, 6); EX(C, 10); EX(C, 11);                                 \
        O = mma32(v0, p0, O); SBAR();                                                                         \
        if (u & 1) { v0 = lds128(av + (u >> 1) * REC + fl); SBAR(); }                                         \
        EX(C, 12); CV(p1, 0, 8); EX(C, 13); CV(p1, 1, 10); EX(C, 14); EX(C, 15);                              \
    } while (0)
#pragma unroll 1
    for (int j = 0; j < 8; j += 2) {
#pragma unroll
        for (int jj = 0; jj < 2; ++jj) {
            const unsigned fl = jj * 32768;
            UNIT(sa, sb, oa, ob, q1b, q2b, 0); UNIT(sb, sa, ob, oa, q1a, q2a, 1); UNIT(sa, sb, oa, ob, q1b, q2b, 2); UNIT(sb, sa, ob, oa, q1a, q2a, 3);
            UNIT(sa, sb, oa, ob, q1b, q2b, 4); UNIT(sb, sa, ob, oa, q1a, q2a, 5); UNIT(sa, sb, oa, ob, q1b, q2b, 6); UNIT(sb, sa, ob, oa, q1a, q2a, 7);
        }
    }
#undef UNIT
#undef EX
#undef CV
    unsigned lo2 = hh ? WS_END : l16 >> 1; PIN(lo2);
    LAS f32x4* mine = (LAS f32x4*)(uintptr_t)xm;
    const LAS f32x4* other = (const LAS f32x4*)(uintptr_t)xo;
    p1[2] = pk2(e[12], e[13]); p1[3] = pk2(e[14], e[15]);
    ob = mma32(v1, p1, ob);
    PIN(ob);
#pragma unroll
    for (int i = 0; i < 3; ++i) mine[64 * i] = (f32x4){oa[4 * i], oa[4 * i + 1], oa[4 * i + 2], oa[4 * i + 3]};
    WAITL_BAR();
    PRIO(3);
    const f32x4 t2 = other[128], t0 = other[0], t1 = other[64];
    SBAR();
    PIN(ob);
    f32x16 o;
#pragma unroll
    for (int c = 0; c < 4; ++c) { o[c] = ob[c] + t0[c]; o[4 + c] = ob[4 + c] + t1[c]; o[8 + c] = ob[8 + c] + t2[c]; }
    auto put = [&](float l) {
        const float sc = __builtin_amdgcn_rcpf(l);
        st128(rs, l16, o1, pk8(o, 0, sc));
        st64(rs, lo2, o2, (u32x2){pk2(o[8] * sc, o[9] * sc), pk2(o[10] * sc, o[11] * sc)});
    };
    const float l = upper(o[8]);
    put(l);
    SBAR();
    const float mag = ((fabsf(o[0]) + fabsf(o[1])) + (fabsf(o[2]) + fabsf(o[3]))) + ((fabsf(o[4]) + fabsf(o[5])) + (fabsf(o[6]) + fabsf(o[7]))) + ((fabsf(o[8]) + fabsf(o[9])) + (fabsf(o[10]) + fabsf(o[11])));
    if (__builtin_expect(__any(!(mag * 0x1p-20f + l < 0x1p100f) || !(l > 0x1p-40f)), 0)) {
        if (hh) q2b[0] = 0u;
        float m = -INFINITY;
#pragma unroll 1
        for (int t = 0; t < 64; ++t) m = fmaxf(m, rowmax16(mma32(ld128(rs, l16, kv + t * REC), q1b, mma32k8(ld64(rs, l8, kv2 + t * REC), q2b, f32x16{}))));
        m = halfmax(m);
        o = f32x16{};
#pragma unroll 1
        for (int t = 0; t < 64; ++t) {
            const u32x4 va = ld128(rs, vg, kv + t * REC) | vfill, vb = ld128(rs, vg, kv + (t + 1) * REC + 768) | vfill;
            f32x16 s = mma32(ld128(rs, l16, kv + t * REC), q1b, mma32k8(ld64(rs, l8, kv2 + t * REC), q2b, f32x16{}));
#pragma unroll
            for (int i = 0; i < 16; ++i) s[i] = __builtin_amdgcn_exp2f(s[i] - m);
            o = mma32(vb, pk8(s, 1), mma32(va, pk8(s, 0), o));
        }
        put(upper(o[8]));
    }
}

DI void role_a(const char* x, const char* gw, char* ws, rsrc_t rs, int w, int lane, int b, int rb, int cg) {
    const int head = (b & 7) * 4 + ((b >> 3) & 3), qb = b >> 5;
    const u64 ep = ldflag(ws, WS_E);
    if (__builtin_expect(cg == 15, 0)) { gate_rows(x, gw, rs, rb); Flag(threadIdx.x == 0, FL + b, mktag(ep)).raise(rs); }
    if (__builtin_expect(w >= 4, 0)) {
        const unsigned i = threadIdx.x - 256, rec = i >> 4, c = (i & 3) == 0 ? 0x3f803f80u : 0u;
        *(LAS u32x4*)(uintptr_t)((rec >> 3) * 32768 + (rec & 7) * REC + 1536 + ((i >> 2) & 3) * 384 + 320 + (i & 3) * 16) = (u32x4){(i & 3) == 2 ? 0x3f80u : c, c, c, c};
        const Flag fq(threadIdx.x == 320, FQ + rb * 16 + cg, mktag(ep));
        WAITVL_BAR();
        fq.raise(rs);
    } else {
        if (cg < 10) qkv_compute<0>(rs, w, lane, rb, cg, b, ep); else if (cg < 15) qkv_compute<1>(rs, w, lane, rb, cg, b, ep);
        WAITV_BAR(0);
    }
    const u64 tag = mktag(ep);
    attention(ws, rs, w, lane, head, qb, tag);
    const Flag fo(threadIdx.x == 0, FO + qb * 32 + head, tag);
    WAITV_BAR(0);
    fo.raise(rs);
    if (b == 0) {
        await(ws, FQ + threadIdx.x, tag);
        BAR();
        if (threadIdx.x == 0) stflag(ws, WS_E, tag);
    }
}

DI void hid_compute(rsrc_t rs, int w, int lane, int rb, int cb16) {
    const unsigned rr = lane & 15, g = lane >> 4, rel = rr * 128 + ((g ^ (rr >> 1)) << 4);
    unsigned xa = w * 4096 + rel, wa = 49152 + rel;
    f32x4 acc[2][5];
#pragma unroll
    for (int i = 0; i < 10; ++i) { acc[i / 5][i % 5] = f32x4{}; PIN(acc[i / 5][i % 5]); }
    PIN(xa); PIN(wa);
    u32x4 f[2][7];
    auto rd = [&](int set, int ks, int i) { f[set][i] = i < 2 ? lds128((xa ^ (ks << 6)) + i * 2048) : lds128((wa ^ (ks << 6)) + (i - 2) * 2048); };
    WAITL_BAR();
    PRIO(3);
#pragma unroll
    for (int i = 0; i < 7; ++i) rd(0, 0, i);
    unsigned sb = 0;
#pragma unroll 2
    for (int s = 0; s < 10; ++s) {
#pragma unroll
        for (int ks = 0; ks < 2; ++ks) {
            if (ks) { WAITL_BAR(); const int d = sb == 2 ? -2 : 1; sb += d; xa += d * 16384; wa += d * 10240; }
            SBAR();
#pragma unroll
            for (int i = 0; i < 10; ++i) { const int m = (i ^ (i >> 1)) & 1; acc[m][i >> 1] = mma16(f[ks][m], f[ks][2 + (i >> 1)], acc[m][i >> 1]); if (i < 7) rd(ks ^ 1, ks ^ 1, i); SBAR(); }
        }
    }
    const unsigned so = WS_AB + ((cb16 >> 1) * 64 + rb * 4 + w) * 10240 + (cb16 & 1) * 5120;
#pragma unroll
    for (int j = 0; j < 5; ++j) st128(rs, lane * 16 + (j & 3) * 1024, so + (j >> 2) * 4096, pk44(acc[0][j], acc[1][j]));
}

DI void hid_load(const char* x, const char* wd, const char* ws, int li, int lane, int rb, int cb16, int b, u64 tag) {
    const int rot = ((rb & 7) * 5) >> 2;
    const unsigned row = lane >> 3, ev = swap23(row) * 1280 + (((lane & 7) ^ (row >> 1)) << 4);
    int left = 10 - rot;
    if (li < 2) {
        const unsigned v1 = (ev ^ 64) + 16 * 1280 - 1024;
        const char* base = x + (rb * 128 + 64 * li) * 1280 + rot * 128;
        unsigned lds = li * 8192;
        auto issue = [&]() {
#pragma unroll
            for (int i = 0; i < 4; ++i) dma2(ev, v1, base + ((i >> 1) * 32 + (i & 1) * 4) * 1280, lds + i * 2048);
            base += --left ? 128 : 128 - 1280; lds = lds >= 32768 ? lds - 32768 : lds + 16384;
        };
        await<8>(ws, FL + b, tag);
#pragma unroll 1
        for (int s = 0; s < 3; ++s) { issue(); if (s == 1) WAITV_BAR(8); }
#pragma unroll 1
        for (int s = 0; s < 7; ++s) { WAITV_BAR(8); issue(); }
        WAITV_BAR(8); WAITV_BAR(0); BAR();
    } else {
        const unsigned o = li - 2;
        unsigned vw[5];
#pragma unroll
        for (int i = 0; i < 5; ++i) { const unsigned p = 5 * o + i; vw[i] = (p < 8 ? (ev ^ ((p & 1) << 6)) + (32 * (p >> 2) + 4 * ((p >> 1) & 1) + 16 * (p & 1)) * 1280 : ((ev & 127) ^ ((p & 1) << 6)) + (8 * p + row) * 1280) + 3072 - (i & 3) * 1024; }
        const char* base = wd + (cb16 & 7) * 80 * 1280 + rot * 128 - 3072;
        unsigned lds = 49152 + o * 5120;
        auto issue = [&]() { dma4(vw[0], vw[1], vw[2], vw[3], base, lds); dma1(vw[4], base, lds + 4096); base += --left ? 128 : 128 - 1280; lds = lds >= 49152 + 20480 ? lds - 20480 : lds + 10240; };
        __builtin_amdgcn_s_sleep(32);
#pragma unroll 1
        for (int s = 0; s < 3; ++s) issue();
        WAITV_BAR(10);
#pragma unroll 1
        for (int s = 0; s < 7; ++s) { WAITV_BAR(5); issue(); }
        WAITV_BAR(5); WAITV_BAR(0); BAR();
    }
}

DI void retile_wo(const char* wo, rsrc_t rs, int f, int lane) {
    const int cbk = f / 100, st = (f - cbk * 100) / 5, tile = f % 5, kh = st < 16 ? st >> 3 : (st >> 1) & 1, i = st < 16 ? st & 7 : 8 + (st & 1);
    const int rr = lane & 15, g = lane >> 4;
    const int e = cbk * 80 + (tile < 4 ? 32 * ((tile >> 1) ^ kh) + 8 * (rr >> 2) + 4 * (tile & 1) + (rr & 3) : 64 + rr);
    const int k0 = i < 8 ? (16 * kh + 2 * i + (g >> 1)) * 20 + 8 * (g & 1) : (16 * kh + 8 * (i - 8) + 2 * g) * 20 + 16, k4 = i < 8 ? k0 + 4 : k0 + 20;
    const unsigned short* p = (const unsigned short*)wo + e;
    u32x4 v;
    v[0] = p[k0 * 640] | ((unsigned)p[(k0 + 1) * 640] << 16); v[1] = p[(k0 + 2) * 640] | ((unsigned)p[(k0 + 3) * 640] << 16);
    v[2] = p[k4 * 640] | ((unsigned)p[(k4 + 1) * 640] << 16); v[3] = p[(k4 + 2) * 640] | ((unsigned)p[(k4 + 3) * 640] << 16);
    st128(rs, lane * 16, WS_WT + f * 1024, v);
}

DI void seq_compute(rsrc_t rs, int w, int lane, int kq, int mt, int nt, int hid, u64 tag) {
    const unsigned rr = lane & 15, g = lane >> 4;
    unsigned ac[2], ab = 49152 + (w >> 1) * 5120 + lane * 16;
    ac[0] = ((w & 1) * 64 + rr) * 128 + ((g ^ (rr >> 1)) << 4); ac[1] = ac[0] ^ 64;
    PIN(ac[0]); PIN(ac[1]); PIN(ab);
    f32x4 acc[4][5];
#pragma unroll
    for (int j = 0; j < 5; ++j) {
        f32x16 z = zero16(); PIN(z);
#pragma unroll
        for (int i = 0; i < 4; ++i) acc[i][j] = (f32x4){z[4 * i], z[4 * i + 1], z[4 * i + 2], z[4 * i + 3]};
    }
    u32x4 cf[4], af[3];
    WAITVL_BAR();
    if (w == 0) Flag(lane == 0, FW + hid, tag).raise(rs);
    PRIO(0);
#pragma unroll
    for (int i = 0; i < 4; ++i) cf[i] = lds128(ac[0] + i * 2048);
    af[0] = lds128(ab); af[1] = lds128(ab + 1024);
    auto step = [&](int k) {
        const int n = (k + 1) % 6;
#pragma unroll
        for (int j = 0; j < 5; ++j) {
            if (j == 3) WAITL_BAR();
            SBAR();
            af[(k * 5 + j + 2) % 3] = lds128(ab + (j < 3 ? (k % 3) * 10240 + (j + 2) * 1024 : (n % 3) * 10240 + (j - 3) * 1024));
            SBAR();
#pragma unroll
            for (int i0 = 0; i0 < 4; ++i0) {
                const int i = (j & 1) ? 3 - i0 : i0;
                acc[i][j] = mma16(af[(k * 5 + j) % 3], cf[i], acc[i][j]);
                if (j == 4) cf[i] = lds128(ac[n & 1] + (n >> 1) * 16384 + i * 2048);
                SBAR();
            }
        }
    };
#pragma unroll 1
    for (int t = 0; t < 5; ++t) {
        if (__builtin_amdgcn_readfirstlane(t) == 2) PRIO(1);
#pragma unroll
        for (int k = 0; k < 6; ++k) step(k);
    }
    step(0); step(1);
    PRIO(3);
    const unsigned sb = WS_P + (kq * 256 + (mt * 2 + (w & 1)) * 8 + nt * 2 + (w >> 1)) * 10240;
    auto part = [&](int i) { return i < 8 ? pk44(acc[i >> 1][2 * (i & 1)], acc[i >> 1][2 * (i & 1) + 1]) : pk44(acc[2 * (i & 1)][4], acc[2 * (i & 1) + 1][4]); };
    if (w == kq) {
#pragma unroll
        for (int i = 0; i < 10; ++i) *(LAS u32x4*)(uintptr_t)(59392 + lane * 16 + i * 1024) = part(i);
    } else {
#pragma unroll
        for (int i = 0; i < 10; ++i) st128(rs, lane * 16 + (i & 3) * 1024, sb + (i >> 2) * 4096, part(i));
    }
}

DI void seq_load(const char* c, const char* ws, int li, int lane, int kq, int mt, int nt, int cb, unsigned fw, u64 tag, rsrc_t rs, u32x4 (&wl)[10]) {
    auto frags = [&]() {
#pragma unroll
        for (int i = 0; i < 10; ++i) wl[i] = ld128(rs, lane * 16 + (i & 3) * 1024, WS_WT + (cb * 100 + 90 + (i & ~3)) * 1024);
    };
    const char* wt = ws + WS_WT + cb * 102400;
    const int rot = (nt & 1) * 8;
    if (li < 2) {
        const unsigned row = lane >> 3, v0 = row * 4096 + (((lane & 7) ^ (row >> 1)) << 4), v1 = (v0 ^ 64) + 8 * 4096 - 1024;
        const char* base = c + (size_t)(mt * 128 + 64 * li) * 4096 + (kq & 1) * 2048 + rot * 128;
        unsigned lds = li * 8192; int left = 16 - rot;
        auto next = [&]() { lds = lds >= 32768 ? lds - 32768 : lds + 16384; };
        auto issue = [&]() {
#pragma unroll
            for (int i = 0; i < 4; ++i) dma2(v0, v1, base + i * 65536, lds + i * 2048);
            base += --left ? 128 : 128 - 2048; next();
        };
#pragma unroll 1
        for (int s = 0; s < 3; ++s) { issue(); if (s == 0) WAITV_BAR(0); }
#pragma unroll 1
        for (int s = 0; s < 13; ++s) { BAR(); WAITV_BAR(8); issue(); }
        frags();
#pragma unroll 1
        for (int s = 0; s < 3; ++s) { BAR(); WAITV_BAR(8); dma4c(lane * 16, wt + lds, lds); dma4c(lane * 16, wt + lds + 4096, lds + 4096); next(); }
    } else {
        const char* bb = ws + WS_AB + (((kq >> 1) * 4 + nt) * 128 + (kq & 1) * 64 + 4 * rot + li - 2) * 5120;
        unsigned lds = 49152 + (li - 2) * 5120; int left = 32 - 2 * rot;
        auto next = [&]() { lds = lds >= 49152 + 20480 ? lds - 20480 : lds + 10240; };
        auto issue = [&]() { dma5c(lane * 16, bb, lds); bb += --left ? 10240 : 10240 - 64 * 5120; next(); };
        await<8>(ws, FA + ((kq & 1) * 8 + (lane >> 3)) * 16 + (kq >> 1) * 8 + 2 * nt + ((lane >> 2) & 1), tag);
#pragma unroll 1
        for (int s = 0; s < 2; ++s) issue();
        WAITV_BAR(5);
        issue();
#pragma unroll 1
        for (int s = 0; s < 20; ++s) { WAITV_BAR(5); issue(); }
        await(ws, fw, tag);
        frags();
        dma1c(lane * 16, wt + 79872 + (li - 2) * 1024, 79872 + (li - 2) * 1024);
#pragma unroll 1
        for (int s = 20; s < 29; ++s) { WAITV_BAR(5); issue(); }
#pragma unroll 1
        for (int s = 0; s < 2; ++s) { WAITV_BAR(5); dma5c(lane * 16, wt + lds + (s ? 0 : 81920 - 69632), lds); next(); }
        WAITV_BAR(5);
    }
}

DI void role_g(const char* x, const char* cr, const char* ci, const char* wr, const char* wi, const char* wo, char* ws, char* out, rsrc_t rs, int w, int lane, int b) {
    u64 tag = mktag(ldflag(ws, WS_E));
    PINS(tag);
    const int xcd = b & 7, idx = b >> 3;
    const int rb = (xcd & 1) * 8 + (idx & 7), cb16 = (xcd >> 1) * 4 + (idx >> 3), hid = rb * 16 + cb16;
    const int kq = xcd >> 1, mt = (xcd & 1) * 8 + (idx >> 2), nt = idx & 3;
    const int rbk = 2 * mt + (kq & 1), cb = 2 * nt + (kq >> 1);
    if (w < 4) hid_compute(rs, w, lane, rb, cb16);
    else hid_load(x, cb16 < 8 ? wr : wi, ws, w - 4, lane, rb, cb16, b, tag);
    PRIO(3);
    const Flag fa(threadIdx.x == 0, FA + hid, tag);
    WAITVL_BAR();
    fa.raise(rs);
    u32x4 wl[10];
    if (w < 4) {
        if (hid < 200) retile_wo(wo, rs, hid * 4 + w, lane);
        seq_compute(rs, w, lane, kq, mt, nt, hid, tag);
#pragma unroll
        for (int i = 0; i < 10; ++i) wl[i] = lds128(69632 + lane * 16 + i * 1024);
    } else seq_load(kq < 2 ? cr : ci, ws, w - 4, lane, kq, mt, nt, cb, lane < 25 ? FW + 25 * cb + lane : FQ + (rbk >> 1) * 16 + 15, tag, rs, wl);
    const unsigned ts = w & 3, kh = w >> 2, g = lane >> 4, tt = rbk * 2 + (ts >> 1), tq = (ts & 1) * 16 + (lane & 15), l16 = lane * 16;
    const float gate = __uint_as_float(ld32(rs, tq * 4, WS_G + tt * 128));
    const Flag fp(threadIdx.x == 0, FP + (mt * 4 + nt) * 4 + kq, tag);
    WAITVL_BAR();
    fp.raise(rs);
    if (w >= 6) { const unsigned a = 69632 + (w - 6) * 5120; dma5c(l16, ws + WS_WT + cb * 102400 + a, a); }
    u32x4 pa[4]; u32x2 pb[4];
    pa[0] = lds128(59392 + (2 * ts + kh) * 1024 + l16); pb[0] = lds64(59392 + (8 + (ts >> 1)) * 1024 + (ts & 1) * 8 + l16);
    if (w == 4) await<8>(ws, FP + (mt * 4 + nt) * 4 + (lane >> 4), tag);
    WAITL_BAR();
    if (w >= 6) { const unsigned a = 59392 + (w - 6) * 5120; dma5c(l16, ws + WS_WT + cb * 102400 + a, a); }
    const unsigned ps = WS_P + (rbk * 8 + cb) * 10240;
#pragma unroll
    for (int q = 1; q < 4; ++q) { pa[q] = ld128(rs, l16, ps + (q ^ kq) * (256 * 10240) + (2 * ts + kh) * 1024); pb[q] = ld64(rs, l16, kh ? WS_END : ps + (q ^ kq) * (256 * 10240) + (8 + (ts >> 1)) * 1024 + (ts & 1) * 8); }
    SBAR();
    PRIO(0);
    SBAR();
    float y[12];
#pragma unroll
    for (int e = 0; e < 4; ++e) { y[2 * e] = (bflo(pa[0][e]) + bflo(pa[1][e])) - (bflo(pa[2][e]) + bflo(pa[3][e])); y[2 * e + 1] = (bfhi(pa[0][e]) + bfhi(pa[1][e])) - (bfhi(pa[2][e]) + bfhi(pa[3][e])); }
#pragma unroll
    for (int e = 0; e < 2; ++e) { y[8 + 2 * e] = (bflo(pb[0][e]) + bflo(pb[1][e])) - (bflo(pb[2][e]) + bflo(pb[3][e])); y[9 + 2 * e] = (bfhi(pb[0][e]) + bfhi(pb[1][e])) - (bfhi(pb[2][e]) + bfhi(pb[3][e])); }
#pragma unroll
    for (int i = 0; i < 12; ++i) { y[i] *= kq < 2 ? gate : -gate; PIN(y[i]); }
    float og = 1.f - gate;
    unsigned aw = kh * 40960 + l16, xm = aw + ts * 3072, xo = (aw ^ 40960) + ts * 3072, vo = g * 512 + tq * 16, vo2 = g * 512 + tq * 8;
    unsigned oa = (tt * 32 + (ts & 1) * 16 + (lane >> 2)) * 1280 + cb * 160 + kh * 64 + (lane & 3) * 16, ob = kh ? 2048 * 1280 : oa + 128 - (lane & 3) * 8, op = (lane & 3) * 64 + (lane & ~3);
    unsigned o1[4], o2 = WS_O2 + (tt * 32 + 16 * kh) * 256;
#pragma unroll
    for (int i = 0; i < 4; ++i) { o1[i] = WS_O1 + (tt * 32 + 16 * kh) * 1024 + i * 4096; PINS(o1[i]); }
    PIN(og); PIN(oa); PIN(ob); PIN(op); PIN(aw); PIN(xm); PIN(xo); PIN(vo); PIN(vo2); PINS(o2);
    if (w == 4) await(ws, FO + (rbk >> 2) * 32 + (lane >> 1), tag);
    WAITV_BAR(0);
    u32x4 of[10];
#pragma unroll
    for (int i = 0; i < 2; ++i) { const u32x2 lo = ld64(rs, vo2 + i * 2048, o2), hi = ld64(rs, vo2 + i * 2048 + 256, o2); of[i] = (u32x4){lo[0], lo[1], hi[0], hi[1]}; }
#pragma unroll
    for (int i = 0; i < 8; ++i) { of[2 + i] = ld128(rs, vo + (i & 1) * 2048, o1[i >> 1]); if (i == 2) { SBAR(); __builtin_amdgcn_s_sleep(3); SBAR(); } }
    SBAR();
    PRIO(3);
    f32x4 acc[5];
#pragma unroll
    for (int n0 = 0; n0 < 50; ++n0) {
        const int n = n0 < 45 ? n0 : 45 + (n0 + 2) % 5;
        acc[n % 5] = mma16(wl[n % 10], of[n / 5], n < 5 ? f32x4{} : acc[n % 5]);
        if (n < 40) wl[n % 10] = lds128(aw + n * 1024);
        if (n == 44) BAR();
        SBAR();
    }
    LAS f32x4* mine = (LAS f32x4*)(uintptr_t)xm;
    const LAS f32x4* other = (const LAS f32x4*)(uintptr_t)xo;
    mine[0] = acc[2]; mine[64] = acc[3]; mine[128] = acc[4];
    WAITL_BAR();
    const f32x4 a0 = acc[0] + other[0], a1 = acc[1] + other[64], a2 = acc[4] + other[128];
    u32x4 r0 = {pk2(og * a0[0] + y[0], og * a0[1] + y[1]), pk2(og * a0[2] + y[2], og * a0[3] + y[3]), pk2(og * a1[0] + y[4], og * a1[1] + y[5]), pk2(og * a1[2] + y[6], og * a1[3] + y[7])};
    u32x2 r1 = {pk2(og * a2[0] + y[8], og * a2[1] + y[9]), pk2(og * a2[2] + y[10], og * a2[3] + y[11])};
#pragma unroll
    for (int i = 0; i < 4; ++i) r0[i] = __builtin_amdgcn_ds_bpermute(op, r0[i]);
#pragma unroll
    for (int i = 0; i < 2; ++i) r1[i] = __builtin_amdgcn_ds_bpermute(op, r1[i]);
    const rsrc_t ro = __builtin_amdgcn_make_buffer_rsrc(out, 0, 2048 * 1280, 0x00020000);
    __builtin_amdgcn_raw_buffer_store_b128(r0, ro, oa, 0, 0);
    __builtin_amdgcn_raw_buffer_store_b64(r1, ro, ob, 0, 0);
}

__global__ __launch_bounds__(512, 4) void fused_mix(const char* x, const char* cr, const char* ci, const char* wr, const char* wi, const char* wqkv, const char* wo, const char* gw, char* ws, char* out) {
    const int lane = threadIdx.x & 63, w = __builtin_amdgcn_readfirstlane(threadIdx.x >> 6);
    const char* pc; asm volatile("s_getpc_b64 %0" : "=s"(pc));
    const rsrc_t rs = __builtin_amdgcn_make_buffer_rsrc(ws, 0, WS_END, 0x00020000);
    const int b = blockIdx.x, rb = (b & 1) * 8 + ((b >> 3) & 7), cg = ((b >> 1) & 3) + 4 * (b >> 6);
    if ((b >> 3) == 32 && w < 4) {
        const unsigned t = *(const GAS unsigned*)(pc + min((unsigned)w * 8192 + lane * 128, CODE_BYTES));
        asm volatile("" :: "v"(t), "s"(x), "s"(ws));
    }
    if (w >= 4 && cg < 15) { PRIO(3); qkv_load(x, wqkv, w - 4, lane, rb, cg); }
    if (b >= 256) { PRIO(2); role_g(x, cr, ci, wr, wi, wo, ws, out, rs, w, lane, b - 256); }
    else { PRIO(3); role_a(x, gw, ws, rs, w, lane, b, rb, cg); }
}

extern "C" void kernel_launch(void* const* d_in, const int* in_sizes, int n_in, void* d_out, int out_size, void* d_ws, size_t ws_size, hipStream_t stream) {
    (void)hipFuncSetAttribute((const void*)fused_mix, hipFuncAttributeMaxDynamicSharedMemorySize, LDS_BYTES);
    fused_mix<<<512, 512, LDS_BYTES, stream>>>((const char*)d_in[0], (const char*)d_in[1], (const char*)d_in[2], (const char*)d_in[3], (const char*)d_in[4],
                                               (const char*)d_in[6], (const char*)d_in[7], (const char*)d_in[8], (char*)d_ws, (char*)d_out);
}
```
